```python
import torch

def _softmax(x, axis=-1):
    return torch.softmax(x, dim=axis)
def _gelu(x):
    return torch.nn.functional.gelu(x, approximate="tanh")
def _silu(x):
    return torch.nn.functional.silu(x)
def _sigmoid(x):
    return torch.sigmoid(x)
def _layernorm(x, axis=-1, eps=1e-5):
    m = x.mean(dim=axis, keepdim=True)
    v = x.var(dim=axis, keepdim=True, unbiased=False)
    return (x - m) / torch.sqrt(v + eps)
def _scatter_add(tgt, idx, src, axis=0):
    return tgt.clone().index_add_(axis, idx, src)
def _segment_sum(src, seg, n):
    out = torch.zeros((n,) + src.shape[1:], dtype=src.dtype)
    return out.index_add_(0, seg, src)
def _pad(x, pw):
    flat = tuple(v for pair in reversed(pw) for v in pair)
    return torch.nn.functional.pad(x, flat)

kernel_name = 'concat_heads__ssm_conv_s4__linear_attn_block__prompt'

def setup_inputs():

    _w = (lambda a: (lambda t: t.to(torch.bfloat16) if t.is_floating_point() else t)(torch.as_tensor(a)))
    def _A_setup():
        B, L, D, N = 1, 2048, 448, 256
        A_re = -(lambda _x: torch.exp(torch.as_tensor(_x)))((lambda *s: torch.randn(*s, dtype=torch.bfloat16) * 3.2)(D, N))
        A_im = torch.pi * (lambda *s: torch.randn(*s, dtype=torch.bfloat16) * 0.05)(D, N)
        B_re = (lambda *s: torch.randn(*s, dtype=torch.bfloat16) * 0.2)(D, N)
        B_im = (lambda *s: torch.randn(*s, dtype=torch.bfloat16) * 0.2)(D, N)
        C_re = (lambda *s: torch.randn(*s, dtype=torch.bfloat16) * 0.2)(D, N)
        C_im = (lambda *s: torch.randn(*s, dtype=torch.bfloat16) * 0.2)(D, N)
        log_dt = (lambda *s: torch.randn(*s, dtype=torch.bfloat16) * 0.05)(D) * 0.5 - 4.0
        Dskip = (lambda *s: torch.randn(*s, dtype=torch.bfloat16) * 0.05)(D)
        return {
            "u": _w((lambda *s: torch.randn(*s, dtype=torch.bfloat16) * 0.05)(B, L, D)),
            "A_re": _w(A_re),
            "A_im": _w(A_im),
            "B_re": _w(B_re),
            "B_im": _w(B_im),
            "C_re": _w(C_re),
            "C_im": _w(C_im),
            "log_dt": _w(log_dt),
            "Dskip": _w(Dskip),
        }
    _A_in = _A_setup()

    def _B_setup():
        B, S, D = 1, 2048, 448
        return {
            "x": _w((lambda *s: torch.randn(*s, dtype=torch.bfloat16) * 0.05)(B, S, D)),
            "wqkv": _w((lambda *s: torch.randn(*s, dtype=torch.bfloat16) * 0.8)(D, 3 * D)),
            "wo": _w((lambda *s: torch.randn(*s, dtype=torch.bfloat16) * 0.1)(D, D)),
        }
    _B_in = _B_setup()

    return {
        "A_u": _A_in["u"],
        "A_A_re": _A_in["A_re"],
        "A_A_im": _A_in["A_im"],
        "A_B_re": _A_in["B_re"],
        "A_B_im": _A_in["B_im"],
        "A_C_re": _A_in["C_re"],
        "A_C_im": _A_in["C_im"],
        "A_log_dt": _A_in["log_dt"],
        "A_Dskip": _A_in["Dskip"],
        "B_x": _B_in["x"],
        "B_wqkv": _B_in["wqkv"],
        "B_wo": _B_in["wo"],
    }

def reference(A_u, A_A_re, A_A_im, A_B_re, A_B_im, A_C_re, A_C_im, A_log_dt, A_Dskip, B_x, B_wqkv, B_wo):
    def _A_ref(u, A_re, A_im, B_re, B_im, C_re, C_im, log_dt, Dskip):
        Bsz, L, D = u.shape
        dt = torch.exp(log_dt)[:, None]
        A = A_re + 1j * A_im
        Bc = B_re + 1j * B_im
        Cc = C_re + 1j * C_im
        logdA = dt * A
        dB = dt * Bc
        l = (lambda x, dt: x.to(dt))(torch.arange(L), A_re.dtype)
        P = torch.exp(logdA[:, :, None] * l[None, None, :])
        K = torch.einsum("dn,dnl,dn->dl", Cc, P, dB).real
        L2 = 2 * L
        Kp = _pad(K, ((0, 0), (0, L)))
        up = (lambda x, *d: x.permute(*d))(u, 0, 2, 1)
        up = _pad(up, ((0, 0), (0, 0), (0, L)))
        Kf = (lambda x, axes=(-2,-1): torch.fft.rfft2(x.float(), dim=axes))(Kp[:, None, :], axes=(-2, -1))
        Uf = (lambda x, axes=(-2,-1): torch.fft.rfft2(x.float(), dim=axes))(up[:, :, None, :], axes=(-2, -1))
        Yf = Uf * Kf[None, :, :, :]
        y = (lambda x, s, axes=(-2,-1): torch.fft.irfft2(x, s=s, dim=axes).to(torch.bfloat16))(Yf, (1, L2), axes=(-2, -1))[:, :, 0, :L]
        y = (lambda x, *d: x.permute(*d))(y, 0, 2, 1) + u * Dskip[None, None, :]
        return y

    def _B_ref(x, wqkv, wo):
        B, S, D = x.shape
        H = 16; Dh = D // H
        qkv = (lambda x, *d: x.permute(*d))((x @ wqkv).reshape(B, S, 3, H, Dh), 2, 0, 3, 1, 4)
        q, k, v = qkv[0], qkv[1], qkv[2]
        phi = lambda t: torch.where(t > 0, t + 1, torch.exp(t)) + 1e-6
        pq, pk = phi(q), phi(k)
        kv = torch.einsum("bhsd,bhse->bhde", pk, v)
        z = torch.einsum("bhsd->bhd", pk)
        num = torch.einsum("bhqd,bhde->bhqe", pq, kv)
        den = torch.einsum("bhqd,bhd->bhq", pq, z)[..., None] + 1e-6
        o = num / den
        return (lambda x, *d: x.permute(*d))(o, 0, 2, 1, 3).reshape(B, S, D) @ wo

    a = _A_ref(A_u, A_A_re, A_A_im, A_B_re, A_B_im, A_C_re, A_C_im, A_log_dt, A_Dskip)
    b = _B_ref(B_x, B_wqkv, B_wo)
    return torch.concatenate([a, b], axis=-1)
```

```cpp
#include <hip/hip_runtime.h>
#include <hip/hip_bf16.h>
#include <stdint.h>

typedef unsigned short u16;
__device__ __forceinline__ float bf2f(u16 x){ return __uint_as_float(((uint32_t)x)<<16); }
__device__ __forceinline__ u16 f2bf(float f){ __hip_bfloat16 b = __float2bfloat16(f); return *(u16*)&b; }
__device__ __forceinline__ float rbf(float f){ return bf2f(f2bf(f)); }

constexpr int L=2048, D=448, N=256, H=16, DH=28;

__global__ void kgen(const u16* Are,const u16* Aim,const u16* Bre,const u16* Bim,const u16* Cre,const u16* Cim,const u16* logdt, float* K){
  int d = blockIdx.x;
  __shared__ float ar[N], ai[N], cr[N], ci[N];
  float dt = rbf(expf(bf2f(logdt[d])));
  int n = threadIdx.x;
  {
    float a=bf2f(Are[d*N+n]), b=bf2f(Aim[d*N+n]);
    ar[n]=dt*a; ai[n]=dt*b;
    float br=dt*bf2f(Bre[d*N+n]), bi=dt*bf2f(Bim[d*N+n]);
    float c1=bf2f(Cre[d*N+n]), c2=bf2f(Cim[d*N+n]);
    cr[n]=c1*br-c2*bi; ci[n]=c1*bi+c2*br;
  }
  __syncthreads();
  for(int l=threadIdx.x;l<L;l+=256){
    float lf = rbf((float)l);
    float s=0;
    for(int k=0;k<N;k++){
      float e=expf(ar[k]*lf); float sn,cs; sincosf(ai[k]*lf,&sn,&cs);
      s += e*(cr[k]*cs - ci[k]*sn);
    }
    K[d*L+l]=s;
  }
}
__global__ void conv(const u16* u,const float* K,const u16* Dskip,u16* out){
  int d = blockIdx.x;
  __shared__ float ks[L], us[L];
  for(int l=threadIdx.x;l<L;l+=256){ ks[l]=K[d*L+l]; us[l]=bf2f(u[l*D+d]); }
  __syncthreads();
  float ds=bf2f(Dskip[d]);
  for(int t=threadIdx.x;t<L;t+=256){
    float s=0;
    for(int j=0;j<=t;j++) s+=ks[t-j]*us[j];
    out[t*(2*D)+d]=f2bf(rbf(s)+rbf(us[t]*ds));
  }
}
__global__ void qkvk(const u16* x,const u16* w,u16* qkv){
  int r=blockIdx.x; __shared__ float xs[D];
  for(int i=threadIdx.x;i<D;i+=256) xs[i]=bf2f(x[r*D+i]);
  __syncthreads();
  for(int c=threadIdx.x;c<3*D;c+=256){
    float s=0; for(int k=0;k<D;k++) s+=xs[k]*bf2f(w[k*3*D+c]);
    float t=rbf(s);
    if(c<2*D){ float p = t>0? rbf(t+1.f): rbf(expf(t)); t=rbf(p+1e-6f);}
    qkv[r*3*D+c]=f2bf(t);
  }
}
__global__ void kvk(const u16* qkv,float* kv){
  int h=blockIdx.x; int i=threadIdx.x; if(i>=DH*(DH+1)) return;
  int dd=i/(DH+1), e=i%(DH+1);
  float s=0;
  for(int r=0;r<L;r++){
    float pk=bf2f(qkv[r*3*D+D+h*DH+dd]);
    float v = e<DH? bf2f(qkv[r*3*D+2*D+h*DH+e]):1.f;
    s+=pk*v;
  }
  kv[h*DH*(DH+1)+i]=rbf(s);
}
__global__ void outk(const u16* qkv,const float* kv,const u16* wo,u16* out){
  int r=blockIdx.x; __shared__ float o[D];
  for(int c=threadIdx.x;c<D;c+=256){
    int h=c/DH,e=c%DH; float num=0,den=0;
    for(int dd=0;dd<DH;dd++){ float pq=bf2f(qkv[r*3*D+h*DH+dd]); num+=pq*kv[h*DH*(DH+1)+dd*(DH+1)+e]; den+=pq*kv[h*DH*(DH+1)+dd*(DH+1)+DH]; }
    num=rbf(num); den=rbf(rbf(den)+1e-6f);
    o[c]=rbf(num/den);
  }
  __syncthreads();
  for(int c=threadIdx.x;c<D;c+=256){
    float s=0; for(int k=0;k<D;k++) s+=o[k]*bf2f(wo[k*D+c]);
    out[r*2*D+D+c]=f2bf(s);
  }
}

extern "C" void kernel_launch(void* const* d_in, const int* in_sizes, int n_in,
                              void* d_out, int out_size, void* d_ws, size_t ws_size,
                              hipStream_t stream) {
  const u16* u=(const u16*)d_in[0];
  float* K=(float*)d_ws;
  u16* qkv=(u16*)((char*)d_ws + (size_t)D*L*4);
  float* kv=(float*)((char*)d_ws + (size_t)D*L*4 + (size_t)L*3*D*2);
  u16* out=(u16*)d_out;
  kgen<<<D,256,0,stream>>>((const u16*)d_in[1],(const u16*)d_in[2],(const u16*)d_in[3],(const u16*)d_in[4],(const u16*)d_in[5],(const u16*)d_in[6],(const u16*)d_in[7],K);
  conv<<<D,256,0,stream>>>(u,K,(const u16*)d_in[8],out);
  qkvk<<<L,256,0,stream>>>((const u16*)d_in[9],(const u16*)d_in[10],qkv);
  kvk<<<H,832,0,stream>>>(qkv,kv);
  outk<<<L,256,0,stream>>>(qkv,kv,(const u16*)d_in[11],out);
}
```

```cpp
#include <hip/hip_runtime.h>
#include <hip/hip_bf16.h>
#include <stdint.h>

typedef unsigned short u16;
typedef unsigned int u32;
typedef unsigned long long u64;
typedef u32 u32x2 __attribute__((ext_vector_type(2)));
typedef u32 u32x4 __attribute__((ext_vector_type(4)));
typedef short s16x4 __attribute__((ext_vector_type(4)));
typedef float f32x2 __attribute__((ext_vector_type(2)));
typedef float f32x4 __attribute__((ext_vector_type(4)));
typedef float f32x16 __attribute__((ext_vector_type(16)));
typedef __bf16 bf16x2 __attribute__((ext_vector_type(2)));
typedef __bf16 bf16x8 __attribute__((ext_vector_type(8)));
#define LAS __attribute__((address_space(3)))
#define GAS __attribute__((address_space(1)))
#define DEVINL __device__ __forceinline__

constexpr u32 WS_EPOCH = 0;
constexpr u32 WS_PQFLAG = 4096;
constexpr u32 WS_UFLAG = 8192;
constexpr u32 WS_KVF = 65536;
constexpr u32 WS_PART = 1u << 20;
constexpr u32 WS_PQ = 3u << 20;
constexpr u32 WS_UT = 5u << 20;
constexpr u32 WS_DBG = 7u << 20;
constexpr u32 WS_BYTES = 8u << 20;
constexpr u64 EPOCH_INC = 0x9E3779B97F4A7C15ull;
constexpr u32 SPIN_CAP = 1u << 14;

constexpr int LDS_BYTES = 160 * 1024;
constexpr int SLOT = 37888;
constexpr int KG_PITCH = 528;
constexpr int KG_WAVE = 54 * KG_PITCH;
constexpr int A_U = 4 * KG_WAVE + 10 * KG_PITCH;
constexpr int A_K = A_U + 128 + 16384;
constexpr int K_PITCH = 4352;
constexpr int A_MISC = A_K + 4 * K_PITCH;
constexpr int A_JUNK = A_MISC + 64;
static_assert(A_U % 16 == 0 && A_K % 16 == 0, "align");
static_assert(A_JUNK + 256 <= LDS_BYTES, "lds");
constexpr int B_JUNK = 4 * SLOT + 64;

struct Params {
  const u16 *u, *Are, *Aim, *Bre, *Bim, *Cre, *Cim, *logdt, *Dskip, *x, *wqkv, *wo;
  u16* out;
  char* ws;
};

DEVINL float bf2f(u32 h) { return __uint_as_float(h << 16); }
DEVINL float bfhi(u32 w) { return __uint_as_float(w & 0xffff0000u); }
DEVINL u32 pk(float a, float b) {
  f32x2 v = {a, b};
  bf16x2 r = __builtin_convertvector(v, bf16x2);
  return __builtin_bit_cast(u32, r);
}
DEVINL f32x4 mfma16(u32x4 a, u32x4 b, f32x4 c) {
  return __builtin_amdgcn_mfma_f32_16x16x32_bf16(__builtin_bit_cast(bf16x8, a), __builtin_bit_cast(bf16x8, b), c, 0, 0, 0);
}
DEVINL f32x16 mfma32(u32x4 a, u32x4 b, f32x16 c) {
  return __builtin_amdgcn_mfma_f32_32x32x16_bf16(__builtin_bit_cast(bf16x8, a), __builtin_bit_cast(bf16x8, b), c, 0, 0, 0);
}
DEVINL u32x2 trd(LAS char* p) {
  s16x4 v = __builtin_amdgcn_ds_read_tr16_b64_v4i16((LAS s16x4*)p);
  return __builtin_bit_cast(u32x2, v);
}
DEVINL u32x4 trd2(LAS char* p0, LAS char* p1) {
  u32x2 a = trd(p0), b = trd(p1);
  return u32x4{a.x, a.y, b.x, b.y};
}
DEVINL void dma16(const void* base, u32 off, u32 lds) {
  asm volatile("s_mov_b32 m0, %2\n\ts_nop 0\n\tglobal_load_lds_dwordx4 %0, %1" ::"v"(off), "s"(base), "s"(lds) : "memory");
}
DEVINL void dma16nt(const void* base, u32 off, u32 lds) {
  asm volatile("s_mov_b32 m0, %2\n\ts_nop 0\n\tglobal_load_lds_dwordx4 %0, %1 nt" ::"v"(off), "s"(base), "s"(lds) : "memory");
}
DEVINL void touch(const void* ptr, u32 lds) {
  asm volatile("s_mov_b32 m0, %1\n\ts_nop 0\n\tglobal_load_lds_dword %0, off" ::"v"(ptr), "s"(lds) : "memory");
}
DEVINL const char* bline(const Params& P, u32 l, u32 xc) {
  if (l >= 7680u) l = 7679u;
  u32 c = l / 1280u, r = l - c * 1280u;
  c += 1;
  u32 r2 = r - 512u, k = r2 / 12u, s = r2 - k * 12u;
  u32 ox = ((xc >> 1) * 512u + r) * 896u + c * 128u;
  u32 ow = (c * 64u + k) * 2688u + (s >> 2) * 896u + (xc & 1) * 384u + (s & 3u) * 128u;
  return r < 512u ? (const char*)P.x + ox : (const char*)P.wqkv + ow;
}
DEVINL void st16(const void* base, u32 off, u32x4 v) {
  asm volatile("global_store_dwordx4 %0, %1, %2 sc1\n\ts_nop 1" ::"v"(off), "v"(v), "s"(base) : "memory");
}
#define WAITV(n) asm volatile("s_waitcnt vmcnt(" #n ")" ::: "memory")
#define BAR() do { asm volatile("s_waitcnt lgkmcnt(0)" ::: "memory"); __builtin_amdgcn_s_barrier(); asm volatile("" ::: "memory"); } while (0)
#define RLX_AGENT __ATOMIC_RELAXED, __HIP_MEMORY_SCOPE_AGENT

DEVINL f32x2 cmul(f32x2 a, f32x2 w) {
  return f32x2{a.x * w.x - a.y * w.y, a.x * w.y + a.y * w.x};
}
DEVINL float phi(float t) {
  float e = __builtin_amdgcn_fmed3f(__builtin_amdgcn_exp2f(t * 1.44269504088896f), 0.f, 1.f);
  return fmaxf(t + 1.f, e) + 1e-6f;
}
DEVINL float ror1(float v) {
  return __int_as_float(__builtin_amdgcn_update_dpp(0, __float_as_int(v), 0x13C, 0xf, 0xf, false));
}

#define STAMP(i)
#define STAMP_OUT()

__global__ void __launch_bounds__(512, 2) fused_kernel(Params P) {
  __shared__ __attribute__((aligned(16))) char smem[LDS_BYTES];
  LAS char* sm = (LAS char*)smem;
  const int tid = threadIdx.x, lane = tid & 63;
  const int wave = __builtin_amdgcn_readfirstlane(tid >> 6);
  const int xc = blockIdx.x & 7, idx = blockIdx.x >> 3;
  STAMP(0);
  const u32 sm0 = (u32)(uintptr_t)sm;
  const auto wsr = __builtin_amdgcn_make_buffer_rsrc((void*)P.ws, 0, WS_BYTES, 0x00020000);
  const u64 T = *(const u64*)(P.ws + WS_EPOCH) + EPOCH_INC;
  const u32 Tlo = (u32)T, Thi = (u32)(T >> 32);
  const int g = lane >> 4, q = (lane >> 2) & 3, p = lane & 3, l15 = lane & 15;

  if (idx < 16) {
    const int rb = (xc >> 1) * 4 + (idx & 3), hp = (xc & 1) * 4 + (idx >> 2);
    const int tg = wave & 3, hd = wave >> 2;
    u32 xoff = (u32)(rb * 128 + wave * 8 + (lane >> 3)) * 896u + (u32)(((lane & 7) ^ ((4 * wave + (lane >> 4)) & 7)) << 4);
    u32 woff[3];
#pragma unroll
    for (int i = 0; i < 3; i++) {
      u32 e = (u32)(i * 512 + tid);
      u32 rho = e / 21u, r = e - rho * 21u, m = r / 7u, pp = r - m * 7u;
      u32 k = (rho & ~7u) | ((rho & 1u) << 2) | ((rho >> 1) & 3u);
      woff[i] = k * 2688u + m * 896u + (u32)hp * 112u + pp * 16u;
    }
    auto issue = [&](int c, int slot) {
      const char* xb = (const char*)P.x + c * 128;
      const char* wb = (const char*)P.wqkv + c * (64 * 2688);
      u32 d = sm0 + slot * SLOT + wave * 1024;
      dma16(xb, xoff, d);
      dma16(xb + 64 * 896, xoff, d + 8192);
      dma16(wb, woff[0], d + 16384);
      dma16(wb, woff[1], d + 24576);
      if (wave < 5) dma16(wb, woff[2], d + 32768);
    };
#pragma unroll 1
    for (int c = 0; c < 4; c++) issue(c, c);

    f32x4 acc[3][2][2];
#pragma unroll
    for (int m = 0; m < 3; m++)
#pragma unroll
      for (int t = 0; t < 2; t++)
#pragma unroll
        for (int tt = 0; tt < 2; tt++) acc[m][t][tt] = f32x4{0.f, 0.f, 0.f, 0.f};
    const int xa0 = (tg * 32 + l15) * 128 + ((g ^ (l15 >> 1)) << 4);
    const int wa0 = 16384 + (8 * g + 2 * q) * 336 + 8 * p + hd * 56;
#pragma unroll 1
    for (int c = 0; c < 7; c++) {
      if (c < 5) WAITV(8); else if (c == 5) WAITV(4); else WAITV(0);
      BAR();
      if (c == 0) STAMP(1);
      if (c >= 1 && c <= 3) issue(c + 3, (c - 1) & 3);
      if (c == 6 && hp < 7) {
        u32 o = (u32)(wave * 8 + (lane >> 3)) * 896u + (u32)hp * 128u + (u32)(((lane & 7) ^ (((lane >> 4) & 3) << 1)) << 4);
#pragma unroll 1
        for (int i = 0; i < 7; i++) dma16((const char*)P.wo + i * (64 * 896), o, sm0 + i * 8192 + wave * 1024);
      }
      LAS char* sb = sm + (c & 3) * SLOT;
#pragma unroll
      for (int ks = 0; ks < 2; ks++) {
        u32x4 xf[2];
#pragma unroll
        for (int tt = 0; tt < 2; tt++) xf[tt] = *(LAS u32x4*)(sb + (xa0 ^ (ks * 64)) + tt * 2048);
#pragma unroll
        for (int m = 0; m < 3; m++)
#pragma unroll
          for (int t = 0; t < 2; t++) {
            LAS char* wp = sb + wa0 + ks * (32 * 336) + m * 112 + t * 32;
            u32x4 wf = trd2(wp, wp + 336);
#pragma unroll
            for (int tt = 0; tt < 2; tt++)
              acc[m][t][tt] = (m == 0) ? mfma16(wf, xf[tt], acc[m][t][tt]) : mfma16(xf[tt], wf, acc[m][t][tt]);
          }
      }
    }
    STAMP(2);
    {
      u32x4 pkf[2], vf[2];
#pragma unroll
      for (int t = 0; t < 2; t++) {
        pkf[t] = u32x4{pk(phi(acc[1][t][0][0]), phi(acc[1][t][0][1])), pk(phi(acc[1][t][0][2]), phi(acc[1][t][0][3])),
                       pk(phi(acc[1][t][1][0]), phi(acc[1][t][1][1])), pk(phi(acc[1][t][1][2]), phi(acc[1][t][1][3]))};
        vf[t] = u32x4{pk(acc[2][t][0][0], acc[2][t][0][1]), pk(acc[2][t][0][2], acc[2][t][0][3]),
                      pk(acc[2][t][1][0], acc[2][t][1][1]), pk(acc[2][t][1][2], acc[2][t][1][3])};
      }
      if (l15 == 12) vf[1] = u32x4{0x3f803f80u, 0x3f803f80u, 0x3f803f80u, 0x3f803f80u};
      LAS char* rbuf = sm + 3 * SLOT + wave * 4096 + lane * 16;
#pragma unroll
      for (int td = 0; td < 2; td++)
#pragma unroll
        for (int te = 0; te < 2; te++)
          *(LAS f32x4*)(rbuf + (td * 2 + te) * 1024) = mfma16(pkf[td], vf[te], f32x4{0.f, 0.f, 0.f, 0.f});
    }
    BAR();
    {
      const int Tt = tid >> 8, ln = (tid >> 2) & 63, w = tid & 3;
      const int i = ln & 15, gg = ln >> 4, td = w >> 1;
      int e = Tt == 0 ? i : ((i & 3) == 3 ? 28 : 16 + 3 * (i >> 2) + (i & 3));
      LAS char* src = sm + 3 * SLOT + ((td * 2 + (e >> 4)) * 64 + 16 * gg + (e & 15)) * 16 + (w & 1) * 8;
      const bool zero = (td == 1 && gg == 3);
#pragma unroll
      for (int h = 0; h < 2; h++) {
        f32x2 s = *(LAS f32x2*)(src + h * 16384);
#pragma unroll
        for (int t = 1; t < 4; t++) s += *(LAS f32x2*)(src + h * 16384 + t * 4096);
        u32 b0 = zero ? 0u : (__float_as_uint(s.x) + 0x800u) >> 12, b1 = zero ? 0u : (__float_as_uint(s.y) + 0x800u) >> 12;
        u32x4 gr = {(Tlo & 0xfff00000u) | b0, Thi, (Tlo & 0xfff00000u) | b1, Thi};
        st16(P.ws + WS_PART + ((hp * 16 + h * 8 + wave) * 16 + rb) * 1024, lane * 16, gr);
      }
    }
    STAMP(3);
#pragma unroll
    for (int tt = 0; tt < 2; tt++) {
      u32x4 f = {pk(phi(acc[0][0][tt][0]), phi(acc[0][0][tt][1])), pk(phi(acc[0][0][tt][2]), phi(acc[0][0][tt][3])),
                 pk(phi(acc[0][1][tt][0]), phi(acc[0][1][tt][1])), pk(phi(acc[0][1][tt][2]), phi(acc[0][1][tt][3]))};
      if (g == 3) { f.z = 0; f.w = 0; }
      st16(P.ws + WS_PQ + ((rb * 8 + tg * 2 + tt) * 16 + hp * 2 + hd) * 1024, lane * 16, f);
    }
    WAITV(0);
    BAR();
    if (tid == 0) __hip_atomic_store((GAS u64*)(P.ws + WS_PQFLAG + rb * 128 + hp * 8), T, RLX_AGENT);
    STAMP(4);
    if (hp < 7) {
      const int hh = hd;
_Pragma("unroll 1") for (u32 spin = 0; spin < SPIN_CAP; spin++) {
        u64 v = __hip_atomic_load((GAS u64*)(P.ws + WS_PQFLAG + rb * 128 + (hh * 4 + (lane & 3)) * 8), RLX_AGENT);
        if (__all(v == T)) break;
        __builtin_amdgcn_s_sleep(1);
      }
      asm volatile("" ::: "memory");
      STAMP(5);
      u32x4 pq[8][2];
#pragma unroll
      for (int h = 0; h < 8; h++)
#pragma unroll
        for (int tt = 0; tt < 2; tt++)
          pq[h][tt] = __builtin_amdgcn_raw_buffer_load_b128(wsr, lane * 16 + (tt * 16 + h) * 1024, WS_PQ + ((rb * 8 + tg * 2) * 16 + hh * 8) * 1024, 16);
_Pragma("unroll 1") for (u32 spin = 0; spin < SPIN_CAP; spin++) {
        asm volatile("" ::: "memory");
        u32x4 gr[8];
#pragma unroll
        for (int i = 0; i < 8; i++) gr[i] = __builtin_amdgcn_raw_buffer_load_b128(wsr, tid * 16, WS_KVF + i * 8192, 16);
        bool ok = true;
#pragma unroll
        for (int i = 0; i < 8; i++) {
          ok &= (gr[i].y == Tlo) & (gr[i].w == Tlo);
          *(LAS u32x2*)(sm + 2 * SLOT + i * 4096 + tid * 8) = u32x2{gr[i].x, gr[i].z};
        }
        if (__all(ok)) break;
        __builtin_amdgcn_s_sleep(2);
      }
      BAR();
      STAMP(6);
      f32x4 oc[4][2];
#pragma unroll
      for (int c = 0; c < 4; c++)
#pragma unroll
        for (int tt = 0; tt < 2; tt++) oc[c][tt] = f32x4{0.f, 0.f, 0.f, 0.f};
      const int r0 = 4 * g + q, r1 = 16 + 3 * g + (q < 2 ? q : 2);
      const int wb0 = hh * (8 * 3584) + r0 * 128 + 8 * p, wb1 = hh * (8 * 3584) + r1 * 128 + 8 * p;
      const int f0 = (r0 >> 1) & 3, f1 = (r1 >> 1) & 3;
      LAS char* kvb = sm + 2 * SLOT + hh * 16384 + lane * 16;
#pragma unroll
      for (int h = 0; h < 8; h++) {
        u32x4 ka0 = *(LAS u32x4*)(kvb + h * 2048), ka1 = *(LAS u32x4*)(kvb + h * 2048 + 1024);
        u32x4 of[2];
#pragma unroll
        for (int tt = 0; tt < 2; tt++) {
          f32x4 n0 = mfma16(ka0, pq[h][tt], f32x4{0.f, 0.f, 0.f, 0.f});
          f32x4 n1 = mfma16(ka1, pq[h][tt], f32x4{0.f, 0.f, 0.f, 0.f});
          float r = __builtin_amdgcn_rcpf(n1[3] + 1e-6f);
          of[tt] = u32x4{pk(n0[0] * r, n0[1] * r), pk(n0[2] * r, n0[3] * r), pk(n1[0] * r, n1[1] * r), pk(n1[2] * r, 0.f)};
        }
#pragma unroll
        for (int c = 0; c < 4; c++) {
          const int cc = c ^ ((h & 1) << 1);
          u32x4 wf = trd2(sm + wb0 + ((cc ^ f0) << 5) + h * 3584, sm + wb1 + ((cc ^ f1) << 5) + h * 3584);
#pragma unroll
          for (int tt = 0; tt < 2; tt++) oc[c][tt] = mfma16(wf, of[tt], oc[c][tt]);
        }
      }
      STAMP(7);
      LAS char* xb = sm + 3 * SLOT;
#pragma unroll
      for (int c = 0; c < 4; c++) *(LAS f32x4*)(xb + wave * 4096 + c * 1024 + lane * 16) = hh ? oc[c][0] : oc[c][1];
      BAR();
      LAS char* tb = sm + wave * 2304;
#pragma unroll
      for (int c = 0; c < 4; c++) {
        f32x4 o = *(LAS f32x4*)(xb + (wave ^ 4) * 4096 + c * 1024 + lane * 16);
        o += hh ? oc[c][1] : oc[c][0];
        *(LAS u32x2*)(tb + l15 * 144 + c * 32 + g * 8) = u32x2{pk(o[0], o[1]), pk(o[2], o[3])};
      }
#pragma unroll
      for (int i = 0; i < 2; i++) {
        int row = i * 8 + (lane >> 3);
        u32x4 v = *(LAS u32x4*)(tb + row * 144 + (lane & 7) * 16);
        *(u32x4*)((char*)P.out + (size_t)(rb * 128 + tg * 32 + hh * 16 + row) * 1792 + 896 + hp * 128 + (lane & 7) * 16) = v;
      }
      STAMP(8);
    }
  } else {
    const int mu = idx - 16;
    if (xc < 7) {
      const int gam = xc;
      if (tid == 0) *(LAS u32*)(sm + A_MISC) = 0;
      if (wave < 4) {
        const int ch = gam * 64 + mu * 4 + wave;
        u32 pr[2][6];
#pragma unroll
        for (int r = 0; r < 2; r++) {
          int o = ch * 128 + r * 64 + lane;
          pr[r][0] = __builtin_nontemporal_load((const u32*)P.Are + o);
          pr[r][1] = __builtin_nontemporal_load((const u32*)P.Aim + o);
          pr[r][2] = __builtin_nontemporal_load((const u32*)P.Bre + o);
          pr[r][3] = __builtin_nontemporal_load((const u32*)P.Bim + o);
          pr[r][4] = __builtin_nontemporal_load((const u32*)P.Cre + o);
          pr[r][5] = __builtin_nontemporal_load((const u32*)P.Cim + o);
        }
        const float ldt = bf2f(P.logdt[ch]), dsk = bf2f(P.Dskip[ch]);
        BAR();
        LAS char* KA = sm + A_K + wave * K_PITCH;
        if (lane < 29) *(LAS u32x2*)(KA + lane * 8) = u32x2{0u, 0u};
        const float dt = bf2f(pk(expf(ldt), 0.f) & 0xffffu);
        LAS char* sg = sm + wave * KG_WAVE;
        f32x16 kacc;
#pragma unroll
        for (int i = 0; i < 16; i++) kacc[i] = 0.f;
#pragma unroll 1
        for (int r = 0; r < 2; r++) {
          u32 v[6];
#pragma unroll
          for (int i = 0; i < 6; i++) v[i] = r ? pr[1][i] : pr[0][i];
          if (r == 0) STAMP(1);
          f32x2 z1[2], z8[2], z256[2], w0[2];
#pragma unroll
          for (int s = 0; s < 2; s++) {
            float f[6];
#pragma unroll
            for (int i = 0; i < 6; i++) f[i] = s ? bfhi(v[i]) : bf2f(v[i] & 0xffffu);
            float ar = (dt * f[0]) * 1.44269504088896f, ai = (dt * f[1]) * 0.159154943091895f;
            float e1 = __builtin_amdgcn_exp2f(ar), e8 = __builtin_amdgcn_exp2f(ar * 8.f), e256 = __builtin_amdgcn_exp2f(ar * 256.f);
            float a1 = __builtin_amdgcn_fractf(ai), a8 = __builtin_amdgcn_fractf(ai * 8.f), a256 = __builtin_amdgcn_fractf(ai * 256.f);
            z1[s] = f32x2{e1 * __builtin_amdgcn_cosf(a1), -e1 * __builtin_amdgcn_sinf(a1)};
            z8[s] = f32x2{e8 * __builtin_amdgcn_cosf(a8), e8 * __builtin_amdgcn_sinf(a8)};
            z256[s] = f32x2{e256 * __builtin_amdgcn_cosf(a256), -e256 * __builtin_amdgcn_sinf(a256)};
            float br = dt * f[2], bi = dt * f[3];
            w0[s] = f32x2{f[4] * br - f[5] * bi, -(f[4] * bi + f[5] * br)};
          }
          auto put = [&](int row, const f32x2* a) {
            *(LAS u32x2*)(sg + row * KG_PITCH + lane * 8) = u32x2{pk(a[0].x, a[0].y), pk(a[1].x, a[1].y)};
          };
          {
            f32x2 c[4][2], s32[2];
            *(LAS u32x2*)(sg + lane * 8) = u32x2{0x3f80u, 0x3f80u};
#pragma unroll
            for (int s = 0; s < 2; s++) {
              c[1][s] = z8[s];
              c[2][s] = cmul(z8[s], z8[s]);
              c[3][s] = cmul(c[2][s], z8[s]);
              s32[s] = cmul(c[2][s], c[2][s]);
              c[0][s] = s32[s];
            }
            put(1, c[1]); put(2, c[2]); put(3, c[3]); put(4, c[0]);
#pragma unroll
            for (int j = 5; j < 32; j++) {
#pragma unroll
              for (int s = 0; s < 2; s++) c[j & 3][s] = cmul(c[j & 3][s], s32[s]);
              put(j, c[j & 3]);
            }
          }
          {
            f32x2 w[8][2];
#pragma unroll
            for (int s = 0; s < 2; s++) w[0][s] = w0[s];
            put(32, w[0]);
#pragma unroll
            for (int k = 1; k < 8; k++) {
#pragma unroll
              for (int s = 0; s < 2; s++) w[k][s] = cmul(w[k - 1][s], z1[s]);
              put(32 + k, w[k]);
            }
#pragma unroll
            for (int k = 0; k < 4; k++) {
#pragma unroll
              for (int s = 0; s < 2; s++) w[2 * k][s] = cmul(w[2 * k][s], z256[s]);
              put(40 + k, w[2 * k]);
            }
#pragma unroll
            for (int k = 0; k < 4; k++) {
#pragma unroll
              for (int s = 0; s < 2; s++) w[(k & 1) * 4][s] = cmul(w[(k & 1) * 4][s], z256[s]);
              put(44 + k, w[(k & 1) * 4]);
            }
            constexpr int rw[5] = {48, 49, 52, 53, 50};
#pragma unroll
            for (int k = 0; k < 5; k++) {
#pragma unroll
              for (int s = 0; s < 2; s++) w[0][s] = cmul(w[0][s], z256[s]);
              put(rw[k], w[0]);
            }
          }
          LAS char* fr = sg + (lane & 31) * KG_PITCH + (lane >> 5) * 16;
#pragma unroll
          for (int ks = 0; ks < 16; ks++) {
            u32x4 a = *(LAS u32x4*)(fr + 32 * KG_PITCH + ks * 32), b = *(LAS u32x4*)(fr + ks * 32);
            kacc = mfma32(a, b, kacc);
          }
        }
        STAMP(2);
        {
          const int j = lane & 31, h = lane >> 5;
          LAS char* kb = KA + 232 + 16 * j;
          if (lane == 0) kacc[0] += dsk;
#pragma unroll
          for (int r = 0; r < 2; r++) {
            u32 vv = pk(kacc[8 + r], kacc[8 + r]);
            LAS char* a = kb + 2 * (1024 + 256 * r) + h * 1024;
            *(LAS u32x4*)(a - 8) = u32x4{vv, vv, vv, vv};
            *(LAS u16*)(a + ((j & 1) ? 0 : 8)) = (u16)vv;
          }
          if (lane == 0) { u32 vv = pk(kacc[10], kacc[10]); *(LAS u32x2*)(KA + 232 + 2 * 2044) = u32x2{vv, vv}; }
          if (h == 1) {
#pragma unroll
            for (int b = 0; b < 2; b++) {
              float a0 = kacc[4 + 2 * b], a1 = kacc[5 + 2 * b];
              u32 aa = pk(a0, a0);
              LAS char* a = kb + 2 * (512 + 256 * b);
              *(LAS u32*)(a - 4) = aa;
              *(LAS u32x2*)(a) = u32x2{aa, pk(a0, a1)};
              *(LAS u32*)(a + 8) = pk(a1, a1);
            }
          }
          asm volatile("" ::: "memory");
          if (h == 0) {
            u32 v00 = pk(kacc[4], kacc[4]);
            LAS char* a = kb + 512;
            *(LAS u16*)(a - 2) = (u16)v00;
            *(LAS u32x2*)(a) = u32x2{v00, pk(kacc[5], kacc[6])};
            *(LAS u32*)(a + 8) = pk(kacc[6], kacc[6]);
            *(LAS u16*)(a + 12) = (u16)pk(kacc[7], kacc[7]);
          }
          asm volatile("" ::: "memory");
          *(LAS u32x2*)(kb + 8 * h) = u32x2{pk(kacc[0], kacc[1]), pk(kacc[2], kacc[3])};
        }
        STAMP(3);
        BAR();
        STAMP(4);
        u32x4 uf[16];
        {
          const int Q0 = 1 + q + p + 8 * g;
          LAS char* ub = sm + A_U + 64 * (Q0 >> 1) + 8 * (Q0 & 1) + 16 * wave;
#pragma unroll
          for (int n = 0; n < 16; n++) uf[n] = trd2(ub + n * 1024, ub + n * 1024 + 128);
        }
        LAS char* kf = KA + 224 + 32 * p - 64 * g - 8 * q;
        LAS char* yb = sm + lane * 8 + wave * 2;
        float c3 = 0.f, c2 = 0.f, c1 = 0.f;
        const bool l63 = lane == 63;
#pragma unroll
        for (int blk = 0; blk < 4; blk++) {
          f32x4 ya[8];
#pragma unroll
          for (int i = 0; i < 8; i++) ya[i] = f32x4{0.f, 0.f, 0.f, 0.f};
#pragma unroll
          for (int dl = 0; dl < 8 * blk + 8; dl++) {
            u32x4 a = trd2(kf + 128 * dl + 32, kf + 128 * dl);
#pragma unroll
            for (int i = 0; i < 8; i++) {
              const int th = 8 * blk + i;
              if (th >= dl && ((th - dl) & 1) == 0) ya[i] = mfma16(a, uf[(th - dl) >> 1], ya[i]);
            }
          }
          float y[8];
#pragma unroll
          for (int i = 0; i < 8; i++) {
            float a3 = ya[i][3];
            float a2 = ya[i][2] + ror1(l63 ? c3 : a3);
            float a1 = ya[i][1] + ror1(l63 ? c2 : a2);
            y[i] = ya[i][0] + ror1(l63 ? c1 : a1);
            c3 = a3; c2 = a2; c1 = a1;
          }
#pragma unroll
          for (int i = 0; i < 8; i += 2) {
            u32 w = pk(y[i], y[i + 1]);
            *(LAS u16*)(yb + (8 * blk + i) * 512) = (u16)w;
            *(LAS u16*)(yb + (8 * blk + i + 1) * 512) = (u16)(w >> 16);
          }
          BAR();
          STAMP(5 + blk);
        }
      } else {
        const int hw = wave - 4;
        const u32 stg = A_U + 128 + hw * 4096;
        {
          u32 o = (u32)(mu * 128 + hw * 32 + (lane >> 3)) * 896u + (u32)gam * 128u + (u32)(lane & 7) * 16u;
#pragma unroll
          for (int i = 0; i < 4; i++) dma16nt((const char*)P.u + i * (8 * 896), o, sm0 + stg + i * 1024);
        }
#pragma unroll
        for (int i = 0; i < 2; i++) touch(bline(P, (u32)((i * 256 + hw * 64 + lane) * 16 + mu), xc), sm0 + A_JUNK);
        if (tid < 256 + 8) *(LAS u32x4*)(sm + A_U + lane * 16) = u32x4{0u, 0u, 0u, 0u};
        BAR();
        WAITV(2);
        STAMP(1);
#pragma unroll
        for (int i = 0; i < 4; i++) {
          LAS char* a = sm + stg + (8 * p + q) * 128 + (4 * i + g) * 8;
          u32x4 v = trd2(a, a + 512);
          st16(P.ws + WS_UT + (gam * 16 + 4 * i) * 16384 + (16 * mu + 4 * hw) * 64, g * 16384 + l15 * 16, v);
        }
        WAITV(0);
        asm volatile("" ::"s"(Tlo), "s"(Thi));
        if (lane == 0) {
          u32 old = __hip_atomic_fetch_add((LAS u32*)(sm + A_MISC), 1u, __ATOMIC_RELAXED, __HIP_MEMORY_SCOPE_WORKGROUP);
          if (old == 3) __hip_atomic_store((GAS u64*)(P.ws + WS_UFLAG + gam * 128 + mu * 8), T, RLX_AGENT);
        }
        STAMP(2);
  _Pragma("unroll 1") for (u32 spin = 0; spin < SPIN_CAP; spin++) {
          u64 v = __hip_atomic_load((GAS u64*)(P.ws + WS_UFLAG + gam * 128 + (hw * 4 + (lane & 3)) * 8), RLX_AGENT);
          if (__all(v == T)) break;
          __builtin_amdgcn_s_sleep(2);
        }
        asm volatile("" ::: "memory");
        STAMP(3);
        u32x4 uv[4];
#pragma unroll
        for (int i = 0; i < 4; i++)
          uv[i] = __builtin_amdgcn_raw_buffer_load_b128(wsr, lane * 16 + i * 1024, WS_UT + (gam * 16 + mu) * 16384 + hw * 4096, 16);
#pragma unroll
        for (int i = 0; i < 4; i++) *(LAS u32x4*)(sm + stg + i * 1024 + lane * 16) = uv[i];
        STAMP(4);
        BAR();
        char* ob = (char*)P.out + (size_t)(hw * 64 + lane) * 1792 + gam * 128 + mu * 8;
#pragma unroll 1
        for (int blk = 0; blk < 4; blk++) {
          BAR();
#pragma unroll
          for (int i = 0; i < 2; i++) {
            int t = blk * 512 + i * 256;
            u32x2 v = *(LAS u32x2*)(sm + (t + hw * 64 + lane) * 8);
            *(u32x2*)(ob + (size_t)t * 1792) = v;
          }
        }
        STAMP(9);
      }
    } else {
      const int sg = mu * 8 + wave;
      GAS u64* fin = (GAS u64*)(P.ws + WS_KVF) + sg * 64 + lane;
      __hip_atomic_store(fin, (u64)(~Tlo) << 32, RLX_AGENT);
      touch(bline(P, (u32)(tid * 16 + mu), 7), sm0 + A_JUNK);
      WAITV(0);
      STAMP(1);
      float s0 = 0.f, s1 = 0.f;
_Pragma("unroll 1") for (u32 spin = 0; spin < SPIN_CAP; spin++) {
        asm volatile("" ::: "memory");
        u32x4 gr[16];
#pragma unroll
        for (int i = 0; i < 16; i++) gr[i] = __builtin_amdgcn_raw_buffer_load_b128(wsr, lane * 16 + (i & 3) * 1024, WS_PART + sg * 16384 + (i >> 2) * 4096, 16);
        u32 bad = 0;
        s0 = 0.f; s1 = 0.f;
#pragma unroll
        for (int i = 0; i < 16; i++) {
          bad |= ((gr[i].x ^ Tlo) >> 20) | ((gr[i].z ^ Tlo) >> 20) | (gr[i].y ^ Thi) | (gr[i].w ^ Thi);
          s0 += __uint_as_float(gr[i].x << 12);
          s1 += __uint_as_float(gr[i].z << 12);
        }
        if (__all(bad == 0)) break;
        __builtin_amdgcn_s_sleep(2);
      }
      STAMP(2);
      __hip_atomic_store(fin, ((u64)Tlo << 32) | pk(s0, s1), RLX_AGENT);
      if (sg == 0) {
  _Pragma("unroll 1") for (u32 spin = 0; spin < SPIN_CAP; spin++) {
          bool ok = true;
#pragma unroll
          for (int i = 0; i < 2; i++) {
            int n = i * 64 + lane;
            ok &= __hip_atomic_load((GAS u64*)(P.ws + WS_PQFLAG + (n >> 3) * 128 + (n & 7) * 8), RLX_AGENT) == T;
            ok &= __hip_atomic_load((GAS u64*)(P.ws + WS_UFLAG + (n < 112 ? n : 0) * 8), RLX_AGENT) == T;
            ok &= (u32)(__hip_atomic_load((GAS u64*)(P.ws + WS_KVF) + n * 64, RLX_AGENT) >> 32) == Tlo;
          }
          if (__all(ok)) break;
          __builtin_amdgcn_s_sleep(4);
        }
        if (lane == 0) __hip_atomic_store((GAS u64*)(P.ws + WS_EPOCH), T, RLX_AGENT);
        STAMP(3);
      }
    }
  }
  STAMP(11);
  STAMP_OUT();
}

extern "C" void kernel_launch(void* const* d_in, const int* in_sizes, int n_in, void* d_out, int out_size, void* d_ws,
                              size_t ws_size, hipStream_t stream) {
  Params P;
  P.u = (const u16*)d_in[0]; P.Are = (const u16*)d_in[1]; P.Aim = (const u16*)d_in[2];
  P.Bre = (const u16*)d_in[3]; P.Bim = (const u16*)d_in[4]; P.Cre = (const u16*)d_in[5]; P.Cim = (const u16*)d_in[6];
  P.logdt = (const u16*)d_in[7]; P.Dskip = (const u16*)d_in[8]; P.x = (const u16*)d_in[9];
  P.wqkv = (const u16*)d_in[10]; P.wo = (const u16*)d_in[11];
  P.out = (u16*)d_out; P.ws = (char*)d_ws;
  fused_kernel<<<256, 512, 0, stream>>>(P);
}
```

```cpp
#include <hip/hip_runtime.h>
#include <hip/hip_bf16.h>
#include <stdint.h>

typedef unsigned short u16;
typedef unsigned int u32;
typedef unsigned long long u64;
typedef u32 u32x2 __attribute__((ext_vector_type(2)));
typedef u32 u32x4 __attribute__((ext_vector_type(4)));
typedef u32 u32x8 __attribute__((ext_vector_type(8)));
typedef u32 u32x16 __attribute__((ext_vector_type(16)));
typedef short s16x4 __attribute__((ext_vector_type(4)));
typedef float f32x2 __attribute__((ext_vector_type(2)));
typedef float f32x4 __attribute__((ext_vector_type(4)));
typedef float f32x16 __attribute__((ext_vector_type(16)));
typedef __bf16 bf16x2 __attribute__((ext_vector_type(2)));
typedef __bf16 bf16x8 __attribute__((ext_vector_type(8)));
#define LAS __attribute__((address_space(3)))
#define GAS __attribute__((address_space(1)))
#define DEVINL __device__ __forceinline__

constexpr u32 WS_EPOCH = 0;
constexpr u32 FS = 128;
constexpr u32 WS_PQFLAG = 16384;
constexpr u32 WS_UFLAG = 32768;
constexpr u32 WS_F2 = 49152;
constexpr u32 WS_KVF = 65536;
constexpr u32 WS_F1 = 131072;
constexpr u32 WS_PART = 1u << 20;
constexpr u32 WS_PQ = 3u << 20;
constexpr u32 WS_UT = 5u << 20;
constexpr u32 WS_DBG = 7u << 20;
constexpr u32 WS_BYTES = 8u << 20;
constexpr u64 EPOCH_INC = 0x9E3779B97F4A7C15ull;
#define PSLEEP 2
constexpr u32 SPIN_CAP = 1u << 14;
#define CODE_KB8 3

constexpr int LDS_BYTES = 160 * 1024;
constexpr int JUNK = LDS_BYTES - 256;
constexpr int MISC = JUNK - 64;
constexpr int SLOT = 37888;
constexpr int KG_PITCH = 528;
constexpr int KG_WAVE = 54 * KG_PITCH;
constexpr int A_U = 4 * KG_WAVE + 10 * KG_PITCH;
constexpr int A_K = A_U + 128 + 16384;
constexpr int K_PITCH = 4352;
static_assert(A_U % 16 == 0 && A_K % 16 == 0, "align");
static_assert(A_K + 4 * K_PITCH <= MISC && 4 * SLOT + 64 <= MISC, "lds");

DEVINL float bf2f(u32 h) { return __uint_as_float(h << 16); }
DEVINL float bfhi(u32 w) { return __uint_as_float(w & 0xffff0000u); }
DEVINL u32 pk2(f32x2 v) { return __builtin_bit_cast(u32, __builtin_convertvector(v, bf16x2)); }
DEVINL u32 pk(float a, float b) { return pk2(f32x2{a, b}); }
DEVINL f32x4 mfma16(u32x4 a, u32x4 b, f32x4 c) {
  return __builtin_amdgcn_mfma_f32_16x16x32_bf16(__builtin_bit_cast(bf16x8, a), __builtin_bit_cast(bf16x8, b), c, 0, 0, 0);
}
DEVINL f32x16 mfma32(u32x4 a, u32x4 b, f32x16 c) {
  return __builtin_amdgcn_mfma_f32_32x32x16_bf16(__builtin_bit_cast(bf16x8, a), __builtin_bit_cast(bf16x8, b), c, 0, 0, 0);
}
DEVINL u32x2 trd(LAS char* p) {
  s16x4 v = __builtin_amdgcn_ds_read_tr16_b64_v4i16((LAS s16x4*)p);
  return __builtin_bit_cast(u32x2, v);
}
DEVINL u32x4 trd2(LAS char* p0, LAS char* p1) {
  u32x2 a = trd(p0), b = trd(p1);
  return u32x4{a.x, a.y, b.x, b.y};
}
typedef const GAS char* gptr;
DEVINL void dma16(gptr base, u32 off, u32 lds) {
  asm volatile("s_mov_b32 m0, %2\n\ts_nop 0\n\tglobal_load_lds_dwordx4 %0, %1" ::"v"(off), "s"(base), "s"(lds) : "memory");
}
DEVINL void dma16nt(gptr base, u32 off, u32 lds) {
  asm volatile("s_mov_b32 m0, %2\n\ts_nop 0\n\tglobal_load_lds_dwordx4 %0, %1 nt" ::"v"(off), "s"(base), "s"(lds) : "memory");
}
DEVINL void touch(gptr ptr, u32 lds) {
  asm volatile("s_mov_b32 m0, %1\n\ts_nop 0\n\tglobal_load_lds_dword %0, off" ::"v"(ptr), "s"(lds) : "memory");
}
DEVINL void st16(gptr base, u32 off, u32x4 v) {
  asm volatile("global_store_dwordx4 %0, %1, %2 sc1\n\ts_nop 1" ::"v"(off), "v"(v), "s"(base) : "memory");
}
#define WAITV(n) asm volatile("s_waitcnt vmcnt(" #n ")" ::: "memory")
#define BAR() do { asm volatile("s_waitcnt lgkmcnt(0)" ::: "memory"); __builtin_amdgcn_s_barrier(); asm volatile("" ::: "memory"); } while (0)
#define SCHED() __builtin_amdgcn_sched_barrier(0)
#define ILV(n, m) do { _Pragma("unroll") for (int i_ = 0; i_ < (n); i_++) { __builtin_amdgcn_sched_group_barrier(0x100, 1, 0); __builtin_amdgcn_sched_group_barrier(0x8, (m), 0); } } while (0)
#define RLX_AGENT __ATOMIC_RELAXED, __HIP_MEMORY_SCOPE_AGENT
DEVINL u64 ld8(gptr p) { return __hip_atomic_load((GAS u64*)p, RLX_AGENT); }
DEVINL void st8(gptr p, u64 v) { __hip_atomic_store((GAS u64*)p, v, RLX_AGENT); }
DEVINL void poll(gptr p, u64 T) {
#pragma unroll 1
  for (u32 spin = 0; spin < SPIN_CAP; spin++) {
    if (__all(ld8(p) == T)) break;
    __builtin_amdgcn_s_sleep(PSLEEP);
  }
  asm volatile("" ::: "memory");
}

struct C2 { f32x2 re, im; };
DEVINL C2 cmul(C2 a, C2 w) { return C2{a.re * w.re - a.im * w.im, a.re * w.im + a.im * w.re}; }
DEVINL float phi(float t) {
  float e = __builtin_amdgcn_fmed3f(__builtin_amdgcn_exp2f(t * 1.44269504088896f), 0.f, 1.f);
  return __builtin_amdgcn_fmed3f(t + 1.f, e, 3.0e38f) + 1e-6f;
}
DEVINL u32x2 phi4(f32x4 a) { return u32x2{pk(phi(a[0]), phi(a[1])), pk(phi(a[2]), phi(a[3]))}; }
DEVINL u32x2 pk4(f32x4 a) { return u32x2{pk(a[0], a[1]), pk(a[2], a[3])}; }
DEVINL u32x4 cat(u32x2 a, u32x2 b) { return u32x4{a.x, a.y, b.x, b.y}; }
DEVINL float ror1(float v) {
  return __int_as_float(__builtin_amdgcn_update_dpp(0, __float_as_int(v), 0x13C, 0xf, 0xf, true));
}
DEVINL u32 bline(u32 l, u32 xc) {
  if (l >= 7680u) l = 7679u;
  u32 c = l / 1280u, r = l - c * 1280u;
  c += 1;
  u32 r2 = (r - 512u) & 1023u, k = r2 / 12u, s = r2 - k * 12u;
  u32 ox = ((xc >> 1) * 512u + r) * 896u + c * 128u + 1u;
  u32 ow = (c * 64u + k) * 2688u + (s >> 2) * 896u + (xc & 1) * 384u + (s & 3u) * 128u;
  return r < 512u ? ox : ow;
}

#define STAMP(i)
#define STAMP_OUT()

__global__ void __launch_bounds__(512, 2)
fused_kernel(const char* a_u, const char* a_Are, const char* a_Aim, const char* a_Bre, const char* a_Bim, const char* a_Cre,
             const char* a_Cim, const char* a_logdt, const char* a_dskip, const char* a_x, const char* a_wqkv,
             const char* a_wo, char* a_out, char* a_ws) {
  __shared__ __attribute__((aligned(16))) char smem[LDS_BYTES];
  LAS char* sm = (LAS char*)smem;
  asm volatile("s_load_dword s97, %0, 0x0\n\ts_load_dword s98, %0, 0x38\n\ts_load_dword s99, %0, 0x6c"
               ::"s"(__builtin_amdgcn_kernarg_segment_ptr()) : "s97", "s98", "s99");
  const int tid = threadIdx.x, lane = tid & 63;
  const int wave = __builtin_amdgcn_readfirstlane(tid >> 6);
  const int xc = blockIdx.x & 7, idx = blockIdx.x >> 3;
  STAMP(0);
  const u32 sm0 = (u32)(uintptr_t)sm;
  if (wave == 7) {
    u64 pc;
    asm volatile("s_getpc_b64 %0" : "=s"(pc));
#pragma unroll
    for (int i = 0; i < CODE_KB8; i++) touch((gptr)pc + i * 8192 + lane * 128, sm0 + JUNK);
  }
  const int g = lane >> 4, q = (lane >> 2) & 3, p = lane & 3, l15 = lane & 15;
  const int lw = wave & 3, mu = idx & 15;
  const int rb = (xc >> 1) * 4 + (idx & 3), hp = (xc & 1) * 4 + ((idx >> 2) & 3);
  u32 pre[7];
  if (idx < 16) {
    pre[6] = (u32)(rb * 128 + lw * 8 + (lane >> 3)) * 896u + (u32)(((lane & 7) ^ ((4 * lw + (lane >> 4)) & 7)) << 4);
#pragma unroll
    for (int i = 0; i < 6; i++) {
      u32 e = (u32)(i * 256 + (tid & 255));
      u32 rho = e / 21u, r = e - rho * 21u, m = (r * 37u) >> 8;
      u32 k = (rho & ~7u) | ((rho & 1u) << 2) | ((rho >> 1) & 3u);
      pre[i] = k * 2688u + m * (896u - 112u) + r * 16u + (u32)hp * 112u;
    }
  } else {
    pre[0] = bline((u32)((tid & 255) * 16 + mu), xc);
    pre[1] = bline((u32)((256 + (tid & 255)) * 16 + mu), xc);
    pre[2] = (u32)(mu * 128 + lw * 32 + (lane >> 3)) * 896u + (u32)xc * 128u + (u32)(lane & 7) * 16u;
    pre[3] = pre[4] = pre[5] = pre[6] = 0;
  }
#pragma unroll
  for (int i = 0; i < 7; i++) asm volatile("" : "+v"(pre[i]));
  asm volatile("" ::"s"(a_u), "s"(a_Are), "s"(a_Aim), "s"(a_Bre), "s"(a_Bim), "s"(a_Cre), "s"(a_Cim), "s"(a_logdt), "s"(a_dskip),
               "s"(a_x), "s"(a_wqkv), "s"(a_wo), "s"(a_out), "s"(a_ws));
  const gptr ws = (gptr)a_ws;
#define EPOCH_LOAD() const u64 ep = ld8(ws + WS_EPOCH)
#define EPOCH_WAIT() const u64 T = (((u64)(u32)__builtin_amdgcn_readfirstlane((u32)(ep >> 32)) << 32) | (u32)__builtin_amdgcn_readfirstlane((u32)ep)) + EPOCH_INC; const u32 Tlo = (u32)T; asm volatile("" :: "s"(T))
#define P_u ((gptr)a_u)
#define P_logdt ((gptr)a_logdt)
#define P_dskip ((gptr)a_dskip)
#define P_x ((gptr)a_x)
#define P_wqkv ((gptr)a_wqkv)
#define P_wo ((gptr)a_wo)
#define P_out ((gptr)a_out)
#define BTOUCH(o) touch((((o) & 1u) ? P_x - 1 : P_wqkv) + (o), sm0 + JUNK)

  if (idx < 16) {
    EPOCH_LOAD();
    if (wave >= 4) {
      gptr xb = P_x, wb = P_wqkv;
      if (tid == 256) *(LAS u32*)(sm + MISC) = 0;
#pragma unroll 1
      for (int c = 0; c < 7; c++) {
        u32 d = sm0 + (c & 3) * SLOT + lw * 1024;
#pragma unroll
        for (int i = 0; i < 4; i++) dma16(xb + i * (32 * 896), pre[6], d + i * 4096);
#pragma unroll
        for (int i = 0; i < 5; i++) dma16(wb, pre[i], d + 16384 + i * 4096);
        if (lw == 0) dma16(wb, pre[5], d + 16384 + 5 * 4096);
        xb += 128; wb += 64 * 2688;
        if (c > 0) { WAITV(9); BAR(); }
        if (c == 1) STAMP(1);
      }
      WAITV(0);
      EPOCH_WAIT();
      BAR();
      STAMP(2);
      if (hp < 7) {
        u32 o = (u32)(lw * 8 + (lane >> 3)) * 896u + (u32)hp * 128u + (u32)(((lane & 7) ^ (((lane >> 4) & 3) << 1)) << 4);
        gptr wo = P_wo;
#pragma unroll 1
        for (int i = 0; i < 14; i++) dma16(wo + i * (32 * 896), o, sm0 + i * 4096 + lw * 1024);
      }
      int srcoff[2]; bool zero[2];
#pragma unroll
      for (int s = 0; s < 2; s++) {
        const int f2 = lw * 128 + s * 64 + lane;
        const int h = f2 >> 8, Tt = (f2 >> 7) & 1, ln = (f2 >> 1) & 63, td = f2 & 1;
        const int i = ln & 15, gg = ln >> 4;
        const int e = Tt == 0 ? i : ((i & 3) == 3 ? 28 : 16 + 3 * (i >> 2) + (i & 3));
        srcoff[s] = 3 * SLOT + h * 8192 + ((td * 2 + (e >> 4)) * 64 + 16 * gg + (e & 15)) * 16;
        zero[s] = (td == 1 && gg == 3);
      }
      BAR();
      STAMP(3);
#pragma unroll
      for (int s = 0; s < 2; s++) {
        f32x4 v = *(LAS f32x4*)(sm + srcoff[s]) + *(LAS f32x4*)(sm + srcoff[s] + 4096);
        u32x2 w = pk4(v);
        if (zero[s]) w = u32x2{0u, 0u};
        st16(ws + WS_PART + ((hp * 16 + lw * 4 + s * 2) * 16 + rb) * 512, (lane >> 5) * 8192 + (lane & 31) * 16, u32x4{w.x, Tlo, w.y, Tlo});
      }
      if (lane == 0) st8(ws + WS_F1 + ((hp * 4 + lw) * 16 + rb) * FS, T);
      STAMP(4);
      if (lw == 0) {
        const auto wsr = __builtin_amdgcn_make_buffer_rsrc((void*)ws, 0, WS_BYTES, 0x00020000);
        poll(ws + WS_F1 + ((hp * 4 + (rb >> 2)) * 16 + l15) * FS, T);
        STAMP(9);
        f32x4 s;
#pragma unroll 1
        for (u32 spin = 0; spin < SPIN_CAP; spin++) {
          asm volatile("" ::: "memory");
          u32x4 gr[8];
#pragma unroll
          for (int i = 0; i < 8; i++) gr[i] = __builtin_amdgcn_raw_buffer_load_b128(wsr, lane * 16 + (i & 3) * 1024, WS_PART + (hp * 16 + rb) * 8192 + (i >> 2) * 4096, 16);
          u32 bad = 0;
          s = f32x4{0.f, 0.f, 0.f, 0.f};
#pragma unroll
          for (int i = 0; i < 8; i++) {
            bad |= (gr[i].y ^ Tlo) | (gr[i].w ^ Tlo);
            s += f32x4{bf2f(gr[i].x), bfhi(gr[i].x), bf2f(gr[i].z), bfhi(gr[i].z)};
          }
          if (__all(bad == 0)) break;
        }
#pragma unroll
        for (int i = 0; i < 4; i++) s[i] += __shfl_xor(s[i], 32);
        u32x2 w = pk4(s);
        if (lane < 32) st16(ws + WS_KVF + (hp * 16 + rb) * 512, lane * 16, u32x4{w.x, Tlo, w.y, Tlo});
        if (lane == 0) st8(ws + WS_F2 + (hp * 16 + rb) * FS, T);
        STAMP(10);
      }
    } else {
      const int t2 = wave & 1, hd = wave >> 1;
      f32x4 acc[3][2][4];
#pragma unroll
      for (int m = 0; m < 3; m++)
#pragma unroll
        for (int t = 0; t < 2; t++)
#pragma unroll
          for (int tt = 0; tt < 4; tt++) acc[m][t][tt] = f32x4{0.f, 0.f, 0.f, 0.f};
      const int xa0 = (t2 * 64 + l15) * 128 + ((g ^ (l15 >> 1)) << 4);
      const int wa0 = 16384 + (8 * g + 2 * q) * 336 + 8 * p + hd * 56;
      u32x4 xA[4], wA[6], xB[4], wB[6];
#pragma unroll
      for (int i = 0; i < 4; i++) xB[i] = u32x4{0u, 0u, 0u, 0u};
#pragma unroll
      for (int i = 0; i < 6; i++) wB[i] = u32x4{0u, 0u, 0u, 0u};
      auto rd = [&](LAS char* sb, int ks, u32x4* xf, u32x4* wf) {
#pragma unroll
        for (int tt = 0; tt < 4; tt++) xf[tt] = *(LAS u32x4*)(sb + (xa0 ^ (ks * 64)) + tt * 2048);
#pragma unroll
        for (int i = 0; i < 6; i++) {
          const int m = (i / 2 + 1) % 3, t = i & 1;
          LAS char* wp = sb + wa0 + ks * (32 * 336) + m * 112 + t * 32;
          wf[i] = trd2(wp, wp + 336);
        }
      };
      auto mm = [&](const u32x4* xf, const u32x4* wf) {
#pragma unroll
        for (int i = 0; i < 6; i++) {
          const int m = (i / 2 + 1) % 3, t = i & 1;
#pragma unroll
          for (int tt = 0; tt < 4; tt++)
            acc[m][t][tt] = (m == 0) ? mfma16(wf[i], xf[tt], acc[m][t][tt]) : mfma16(xf[tt], wf[i], acc[m][t][tt]);
        }
      };
#pragma unroll 1
      for (int c = 0; c < 7; c++) {
        BAR();
        if (c == 0) STAMP(1);
        LAS char* sb = sm + (c & 3) * SLOT;
        rd(sb, 0, xA, wA);
        mm(xB, wB);
        ILV(16, 1);
        SCHED();
        rd(sb, 1, xB, wB);
        mm(xA, wA);
        ILV(16, 1);
        SCHED();
      }
      STAMP(2);
      mm(xB, wB);
      {
        LAS char* rbuf = sm + 3 * SLOT + wave * 4096 + lane * 16;
        const u32x4 ones = {0x3f803f80u, 0x3f803f80u, 0x3f803f80u, 0x3f803f80u};
        u32x4 pkf[2][2], vf[2][2];
#pragma unroll
        for (int t = 0; t < 2; t++)
#pragma unroll
          for (int k2 = 0; k2 < 2; k2++) {
            pkf[t][k2] = cat(phi4(acc[1][t][2 * k2]), phi4(acc[1][t][2 * k2 + 1]));
            vf[t][k2] = cat(pk4(acc[2][t][2 * k2]), pk4(acc[2][t][2 * k2 + 1]));
            if (t == 1 && l15 == 12) vf[t][k2] = ones;
          }
#pragma unroll
        for (int td = 0; td < 2; td++)
#pragma unroll
          for (int te = 0; te < 2; te++)
            *(LAS f32x4*)(rbuf + (td * 2 + te) * 1024) = mfma16(pkf[td][1], vf[te][1], mfma16(pkf[td][0], vf[te][0], f32x4{0.f, 0.f, 0.f, 0.f}));
      }
      BAR();
      STAMP(3);
#pragma unroll
      for (int tt = 0; tt < 4; tt++) {
        u32x4 f = cat(phi4(acc[0][0][tt]), phi4(acc[0][1][tt]));
        if (g == 3) { f.z = 0; f.w = 0; }
        st16(ws + WS_PQ + ((rb * 8 + t2 * 4 + tt) * 16 + hp * 2 + hd) * 1024, lane * 16, f);
      }
      STAMP(9);
      WAITV(0);
      EPOCH_WAIT();
      (void)Tlo;
      if (lane == 0) {
        u32 old = __hip_atomic_fetch_add((LAS u32*)(sm + MISC), 1u, __ATOMIC_RELAXED, __HIP_MEMORY_SCOPE_WORKGROUP);
        if (old == 3) st8(ws + WS_PQFLAG + (rb * 8 + hp) * FS, T);
      }
      STAMP(4);
    }
    if (hp < 7) {
      EPOCH_WAIT();
      const auto wsr = __builtin_amdgcn_make_buffer_rsrc((void*)ws, 0, WS_BYTES, 0x00020000);
      const int tg = wave & 3, hh = wave >> 2;
      poll(ws + WS_PQFLAG + (rb * 8 + hh * 4 + p) * FS, T);
      STAMP(5);
      u32x4 pq[8][2];
#pragma unroll
      for (int h = 0; h < 8; h++)
#pragma unroll
        for (int tt = 0; tt < 2; tt++)
          pq[h][tt] = __builtin_amdgcn_raw_buffer_load_b128(wsr, lane * 16 + (h & 3) * 1024, WS_PQ + ((rb * 8 + tg * 2 + tt) * 16 + hh * 8 + (h & 4)) * 1024, 16);
      poll(ws + WS_F2 + (wave * 16 + l15) * FS, T);
      STAMP(6);
#pragma unroll 1
      for (u32 spin = 0; spin < SPIN_CAP; spin++) {
        asm volatile("" ::: "memory");
        u32x4 gr[8];
#pragma unroll
        for (int i = 0; i < 8; i++) gr[i] = __builtin_amdgcn_raw_buffer_load_b128(wsr, lane * 16 + (i & 3) * 1024, WS_KVF + wave * 8192 + (i >> 2) * 4096, 16);
        u32 bad = 0;
#pragma unroll
        for (int i = 0; i < 8; i++) {
          bad |= (gr[i].y ^ Tlo) | (gr[i].w ^ Tlo);
          *(LAS u32x2*)(sm + 2 * SLOT + wave * 4096 + i * 512 + lane * 8) = u32x2{gr[i].x, gr[i].z};
        }
        if (__all(bad == 0)) break;
      }
      BAR();
      STAMP(7);
      f32x4 oc[4][2];
#pragma unroll
      for (int c = 0; c < 4; c++)
#pragma unroll
        for (int tt = 0; tt < 2; tt++) oc[c][tt] = f32x4{0.f, 0.f, 0.f, 0.f};
      const int r0 = 4 * g + q, r1 = 16 + 3 * g + (q < 2 ? q : 2);
      const int wb0 = hh * (8 * 3584) + r0 * 128 + 8 * p, wb1 = hh * (8 * 3584) + r1 * 128 + 8 * p;
      const int f0 = (r0 >> 1) & 3, f1 = (r1 >> 1) & 3;
      LAS char* kvb = sm + 2 * SLOT + hh * 16384 + lane * 16;
      u32x4 ka0[8], ka1[8];
#pragma unroll
      for (int h = 0; h < 8; h++) { ka0[h] = *(LAS u32x4*)(kvb + h * 2048); ka1[h] = *(LAS u32x4*)(kvb + h * 2048 + 1024); }
      u32x4 wf[2][4];
      auto ldw = [&](int h) {
#pragma unroll
        for (int c = 0; c < 4; c++) {
          const int cc = c ^ ((h & 1) << 1);
          wf[h & 1][c] = trd2(sm + wb0 + ((cc ^ f0) << 5) + h * 3584, sm + wb1 + ((cc ^ f1) << 5) + h * 3584);
        }
      };
      f32x4 n0[2][2], n1[2][2];
      auto st1 = [&](int h) {
#pragma unroll
        for (int tt = 0; tt < 2; tt++) {
          n0[h & 1][tt] = mfma16(ka0[h], pq[h][tt], f32x4{0.f, 0.f, 0.f, 0.f});
          n1[h & 1][tt] = mfma16(ka1[h], pq[h][tt], f32x4{0.f, 0.f, 0.f, 0.f});
        }
      };
      ldw(0);
      st1(0);
      SCHED();
#pragma unroll
      for (int h = 0; h < 8; h++) {
        if (h < 7) { ldw(h + 1); st1(h + 1); }
        u32x4 of[2];
#pragma unroll
        for (int tt = 0; tt < 2; tt++) {
          f32x4 a = n0[h & 1][tt], b = n1[h & 1][tt];
          float r = __builtin_amdgcn_rcpf(b[3] + 1e-6f);
          of[tt] = u32x4{pk(a[0] * r, a[1] * r), pk(a[2] * r, a[3] * r), pk(b[0] * r, b[1] * r), pk(b[2] * r, 0.f)};
        }
#pragma unroll
        for (int c = 0; c < 4; c++)
#pragma unroll
          for (int tt = 0; tt < 2; tt++) oc[c][tt] = mfma16(wf[h & 1][c], of[tt], oc[c][tt]);
        SCHED();
      }
      STAMP(8);
      LAS char* xb = sm + 3 * SLOT;
#pragma unroll
      for (int c = 0; c < 4; c++) *(LAS f32x4*)(xb + wave * 4096 + c * 1024 + lane * 16) = hh ? oc[c][0] : oc[c][1];
      BAR();
      LAS char* tb = sm + wave * 2304;
#pragma unroll
      for (int c = 0; c < 4; c++) {
        f32x4 o = *(LAS f32x4*)(xb + (wave ^ 4) * 4096 + c * 1024 + lane * 16);
        o += hh ? oc[c][1] : oc[c][0];
        *(LAS u32x2*)(tb + l15 * 144 + c * 32 + g * 8) = pk4(o);
      }
#pragma unroll
      for (int i = 0; i < 2; i++) {
        int row = i * 8 + (lane >> 3);
        u32x4 v = *(LAS u32x4*)(tb + row * 144 + (lane & 7) * 16);
        *(GAS u32x4*)(P_out + (u32)((rb * 128 + tg * 32 + hh * 16 + row) * 1792 + 896 + hp * 128 + (lane & 7) * 16)) = v;
      }
    }
    STAMP(11);
    STAMP_OUT();
  } else {
    if (xc < 7) {
      const int gam = xc;
      if (wave < 4) {
        const int ch = gam * 64 + mu * 4 + wave;
        u32 pr[2][6];
        const char* const par[6] = {a_Are, a_Aim, a_Bre, a_Bim, a_Cre, a_Cim};
#pragma unroll
        for (int r = 0; r < 2; r++)
#pragma unroll
          for (int i = 0; i < 6; i++) pr[r][i] = __builtin_nontemporal_load((const GAS u32*)((gptr)par[i] + (u32)(ch * 512 + r * 256 + lane * 4)));
        const float ldt = bf2f(*(const GAS u16*)(P_logdt + ch * 2)), dsk = bf2f(*(const GAS u16*)(P_dskip + ch * 2));
        LAS char* KA = sm + A_K + wave * K_PITCH;
        if (lane < 29) *(LAS u32x2*)(KA + lane * 8) = u32x2{0u, 0u};
        BAR();
        const float dt = bf2f(pk(expf(ldt), 0.f) & 0xffffu);
        LAS char* sg = sm + wave * KG_WAVE;
        f32x16 kacc;
#pragma unroll
        for (int i = 0; i < 16; i++) kacc[i] = 0.f;
#pragma unroll 1
        for (int r = 0; r < 2; r++) {
          f32x2 f[6];
#pragma unroll
          for (int i = 0; i < 6; i++) { u32 v = r ? pr[1][i] : pr[0][i]; f[i] = f32x2{bf2f(v), bfhi(v)}; }
          if (r == 0) STAMP(1);
          const f32x2 ar = (f[0] * dt) * 1.44269504088896f, ai = (f[1] * dt) * 0.159154943091895f;
          auto zpow = [&](float m, float sgn) {
            f32x2 er = ar * m, an = ai * m;
            f32x2 e = {__builtin_amdgcn_exp2f(er.x), __builtin_amdgcn_exp2f(er.y)};
            float a0 = __builtin_amdgcn_fractf(an.x), a1 = __builtin_amdgcn_fractf(an.y);
            f32x2 cs = {__builtin_amdgcn_cosf(a0), __builtin_amdgcn_cosf(a1)}, sn = {__builtin_amdgcn_sinf(a0), __builtin_amdgcn_sinf(a1)};
            return C2{e * cs, sgn * (e * sn)};
          };
          const C2 z8 = zpow(8.f, 1.f), z1 = zpow(1.f, -1.f), z256 = zpow(256.f, -1.f);
          const f32x2 br = f[2] * dt, bi = f[3] * dt;
          auto put = [&](int row, const C2& a) { *(LAS u32x2*)(sg + row * KG_PITCH + lane * 8) = u32x2{pk2(a.re), pk2(a.im)}; };
          C2 c[4], w[8];
          *(LAS u32x2*)(sg + lane * 8) = u32x2{0x3f803f80u, 0u};
          c[1] = z8;
          c[2] = cmul(z8, z8);
          c[3] = cmul(c[2], z8);
          const C2 s32 = cmul(c[2], c[2]);
          c[0] = s32;
          put(1, c[1]); put(2, c[2]); put(3, c[3]); put(4, c[0]);
#pragma unroll
          for (int j = 5; j < 32; j++) { c[j & 3] = cmul(c[j & 3], s32); put(j, c[j & 3]); }
          w[0] = C2{f[4] * br - f[5] * bi, -(f[4] * bi + f[5] * br)};
          put(32, w[0]);
#pragma unroll
          for (int k = 1; k < 8; k++) { w[k] = cmul(w[k - 1], z1); put(32 + k, w[k]); }
#pragma unroll
          for (int k = 0; k < 4; k++) { w[2 * k] = cmul(w[2 * k], z256); put(40 + k, w[2 * k]); }
#pragma unroll
          for (int k = 0; k < 4; k++) { w[(k & 1) * 4] = cmul(w[(k & 1) * 4], z256); put(44 + k, w[(k & 1) * 4]); }
          constexpr int rw[5] = {48, 49, 52, 53, 50};
#pragma unroll
          for (int k = 0; k < 5; k++) { w[0] = cmul(w[0], z256); put(rw[k], w[0]); }
          SCHED();
          LAS char* fr = sg + (lane & 31) * KG_PITCH + (lane >> 5) * 16;
          u32x4 fa[16], fb[16];
#pragma unroll
          for (int ks = 0; ks < 16; ks++) { fa[ks] = *(LAS u32x4*)(fr + 32 * KG_PITCH + ks * 32); fb[ks] = *(LAS u32x4*)(fr + ks * 32); }
          SCHED();
#pragma unroll
          for (int ks = 0; ks < 16; ks++) kacc = mfma32(fa[ks], fb[ks], kacc);
        }
        STAMP(2);
        {
          const int j = lane & 31, h = lane >> 5;
          LAS char* kb_ = KA + 232 + 16 * j;
          if (lane == 0) kacc[0] += dsk;
#pragma unroll
          for (int r = 0; r < 2; r++) {
            u32 vv = pk(kacc[8 + r], kacc[8 + r]);
            LAS char* a = kb_ + 2 * (1024 + 256 * r) + h * 1024;
            *(LAS u32x4*)(a - 8) = u32x4{vv, vv, vv, vv};
            *(LAS u16*)(a + ((j & 1) ? 0 : 8)) = (u16)vv;
          }
          if (lane == 0) { u32 vv = pk(kacc[10], kacc[10]); *(LAS u32x2*)(KA + 232 + 2 * 2044) = u32x2{vv, vv}; }
          if (h == 1) {
#pragma unroll
            for (int b = 0; b < 2; b++) {
              float a0 = kacc[4 + 2 * b], a1 = kacc[5 + 2 * b];
              u32 aa = pk(a0, a0);
              LAS char* a = kb_ + 2 * (512 + 256 * b);
              *(LAS u32*)(a - 4) = aa;
              *(LAS u32x2*)(a) = u32x2{aa, pk(a0, a1)};
              *(LAS u32*)(a + 8) = pk(a1, a1);
            }
          }
          asm volatile("" ::: "memory");
          if (h == 0) {
            u32 v00 = pk(kacc[4], kacc[4]);
            LAS char* a = kb_ + 512;
            *(LAS u16*)(a - 2) = (u16)v00;
            *(LAS u32x2*)(a) = u32x2{v00, pk(kacc[5], kacc[6])};
            *(LAS u32*)(a + 8) = pk(kacc[6], kacc[6]);
            *(LAS u16*)(a + 12) = (u16)pk(kacc[7], kacc[7]);
          }
          asm volatile("" ::: "memory");
          *(LAS u32x2*)(kb_ + 8 * h) = u32x2{pk(kacc[0], kacc[1]), pk(kacc[2], kacc[3])};
        }
        STAMP(3);
        BAR();
        STAMP(4);
        LAS char* kf = KA + 224 + 32 * p - 64 * g - 8 * q;
        u32x4 kfr[2][4];
        auto ldk = [&](int grp, int buf) {
#pragma unroll
          for (int i = 0; i < 4; i++) kfr[buf][i] = trd2(kf + 128 * (4 * grp + i) + 32, kf + 128 * (4 * grp + i));
        };
        u32x4 uf[16];
        {
          const int Q0 = 1 + q + p + 8 * g;
          LAS char* ub = sm + A_U + 64 * (Q0 >> 1) + 8 * (Q0 & 1) + 16 * wave;
          ldk(0, 0);
#pragma unroll
          for (int n = 0; n < 16; n++) uf[n] = trd2(ub + n * 1024, ub + n * 1024 + 128);
        }
        LAS char* yb = sm + lane * 8 + wave * 2;
        float c3 = 0.f, c2 = 0.f, c1 = 0.f;
        const bool l63 = lane == 63;
        f32x4 ya[2][8];
        auto epi = [&](int blk, int i) {
          float y[2];
#pragma unroll
          for (int k = 0; k < 2; k++) {
            f32x4 d = ya[blk & 1][i + k];
            float a3 = d[3];
            float a2 = d[2] + ror1(l63 ? c3 : a3);
            float a1 = d[1] + ror1(l63 ? c2 : a2);
            y[k] = d[0] + ror1(l63 ? c1 : a1);
            c3 = a3; c2 = a2; c1 = a1;
          }
          u32 w = pk(y[0], y[1]);
          *(LAS u16*)(yb + (8 * blk + i) * 512) = (u16)w;
          *(LAS u16*)(yb + (8 * blk + i + 1) * 512) = (u16)(w >> 16);
        };
        SCHED();
        int buf = 0;
#pragma unroll
        for (int blk = 0; blk < 4; blk++) {
          const int ng = 2 * blk + 2;
#pragma unroll
          for (int gr = 0; gr < ng; gr++) {
            if (gr + 1 < ng) ldk(gr + 1, buf ^ 1); else if (blk < 3) ldk(0, buf ^ 1);
#pragma unroll
            for (int k = 0; k < 4; k++) {
              const int dl = 4 * gr + k;
#pragma unroll
              for (int i = 0; i < 8; i++) {
                const int th = 8 * blk + i;
                if (th >= dl && ((th - dl) & 1) == 0)
                  ya[blk & 1][i] = mfma16(kfr[buf][k], uf[(th - dl) >> 1], dl < 2 ? f32x4{0.f, 0.f, 0.f, 0.f} : ya[blk & 1][i]);
              }
            }
            if (blk > 0 && gr < 2) { epi(blk - 1, 4 * gr); epi(blk - 1, 4 * gr + 2); }
#pragma unroll
            for (int i_ = 0; i_ < 16; i_++) {
              __builtin_amdgcn_sched_group_barrier(0x8, 1, 0);
              if (i_ < 8) __builtin_amdgcn_sched_group_barrier(0x100, 1, 0);
              __builtin_amdgcn_sched_group_barrier(0x2, 2, 0);
            }
            SCHED();
            if (blk > 0 && gr == 1) { BAR(); STAMP(4 + blk); }
            buf ^= 1;
          }
        }
#pragma unroll
        for (int i = 0; i < 8; i += 2) epi(3, i);
        BAR();
        STAMP(8);
        STAMP(11);
        STAMP_OUT();
      } else {
        const int hw = wave - 4;
        const u32 stg = A_U + 128 + hw * 4096;
        EPOCH_LOAD();
#pragma unroll
        for (int i = 0; i < 4; i++) dma16nt(P_u + i * (8 * 896), pre[2], sm0 + stg + i * 1024);
        BTOUCH(pre[0]);
        BTOUCH(pre[1]);
        if (tid < 256 + 8) *(LAS u32x4*)(sm + A_U + lane * 16) = u32x4{0u, 0u, 0u, 0u};
        if (tid == 256) *(LAS u32*)(sm + MISC) = 0;
        BAR();
        WAITV(2);
        STAMP(1);
#pragma unroll
        for (int i = 0; i < 4; i++) {
          LAS char* a = sm + stg + (8 * p + q) * 128 + (4 * i + g) * 8;
          st16(ws + WS_UT + (gam * 16 + 4 * i) * 16384 + (16 * mu + 4 * hw) * 64, g * 16384 + l15 * 16, trd2(a, a + 512));
        }
        WAITV(0);
        EPOCH_WAIT();
        (void)Tlo;
        if (lane == 0) {
          u32 old = __hip_atomic_fetch_add((LAS u32*)(sm + MISC), 1u, __ATOMIC_RELAXED, __HIP_MEMORY_SCOPE_WORKGROUP);
          if (old == 3) st8(ws + WS_UFLAG + (gam * 16 + mu) * FS, T);
        }
        STAMP(2);
        poll(ws + WS_UFLAG + (gam * 16 + hw * 4 + p) * FS, T);
        STAMP(3);
        const auto wsr = __builtin_amdgcn_make_buffer_rsrc((void*)ws, 0, WS_BYTES, 0x00020000);
        u32x4 uv[4];
#pragma unroll
        for (int i = 0; i < 4; i++)
          uv[i] = __builtin_amdgcn_raw_buffer_load_b128(wsr, lane * 16 + i * 1024, WS_UT + (gam * 16 + mu) * 16384 + hw * 4096, 16);
#pragma unroll
        for (int i = 0; i < 4; i++) *(LAS u32x4*)(sm + stg + i * 1024 + lane * 16) = uv[i];
        STAMP(4);
        BAR();
        u32 oo = (u32)(hw * 64 + lane) * 1792u + gam * 128 + mu * 8;
#pragma unroll 1
        for (int blk = 0; blk < 4; blk++) {
          BAR();
#pragma unroll
          for (int i = 0; i < 2; i++) {
            int t = blk * 512 + i * 256;
            u32x2 v = *(LAS u32x2*)(sm + (t + hw * 64 + lane) * 8);
            *(GAS u32x2*)(P_out + (oo + (u32)t * 1792u)) = v;
          }
        }
        STAMP(9);
        STAMP(11);
        STAMP_OUT();
      }
    } else {
      const u32 o = wave < 4 ? pre[0] : pre[1];
      BTOUCH(o);
      if (mu == 0 && wave == 0) {
        EPOCH_LOAD();
        EPOCH_WAIT();
        (void)Tlo;
#pragma unroll 1
        for (int i = 0; i < 4; i++) {
          int n = (i & 1) * 64 + lane;
          u32 o = (i < 2) ? WS_UFLAG + (n < 112 ? n : 0) * FS : WS_PQFLAG + n * FS;
#pragma unroll 1
          for (u32 spin = 0; spin < SPIN_CAP; spin++) {
            if (__all(ld8(ws + o) == T)) break;
            __builtin_amdgcn_s_sleep(8);
          }
        }
        if (lane == 0) st8(ws + WS_EPOCH, T);
      }
      WAITV(0);
      STAMP(11);
      STAMP_OUT();
    }
  }
}

extern "C" void kernel_launch(void* const* d_in, const int* in_sizes, int n_in, void* d_out, int out_size, void* d_ws,
                              size_t ws_size, hipStream_t stream) {
  typedef const char* cp;
  fused_kernel<<<256, 512, 0, stream>>>((cp)d_in[0], (cp)d_in[1], (cp)d_in[2], (cp)d_in[3], (cp)d_in[4], (cp)d_in[5], (cp)d_in[6],
                                        (cp)d_in[7], (cp)d_in[8], (cp)d_in[9], (cp)d_in[10], (cp)d_in[11], (char*)d_out, (char*)d_ws);
}
```

```cpp
#define A_KV 157
#define A_RED 28
#include <hip/hip_runtime.h>
#include <hip/hip_bf16.h>
#include <stdint.h>

typedef unsigned short u16;
typedef unsigned int u32;
typedef unsigned long long u64;
typedef u32 u32x2 __attribute__((ext_vector_type(2)));
typedef u32 u32x4 __attribute__((ext_vector_type(4)));
typedef short s16x2 __attribute__((ext_vector_type(2)));
typedef short s16x4 __attribute__((ext_vector_type(4)));
typedef float f32x2 __attribute__((ext_vector_type(2)));
typedef float f32x4 __attribute__((ext_vector_type(4)));
typedef float f32x16 __attribute__((ext_vector_type(16)));
typedef __bf16 bf16x2 __attribute__((ext_vector_type(2)));
typedef __bf16 bf16x8 __attribute__((ext_vector_type(8)));
#define LAS __attribute__((address_space(3)))
#define GAS __attribute__((address_space(1)))
#define DEVINL __device__ __forceinline__
#if __HIP_DEVICE_COMPILE__
#pragma clang attribute push(__attribute__((target("no-packed-fp32-ops"))), apply_to = function)
#endif
#define __all(c) (__builtin_amdgcn_ballot_w64(!(c)) == 0)
#define __any(c) (__builtin_amdgcn_ballot_w64(c) != 0)
#define __uint_as_float(x) __builtin_bit_cast(float, (u32)(x))

constexpr u32 WS_EPOCH = 0;
constexpr u32 FS = 128;
constexpr u32 WS_FLG = 16384;
constexpr u32 WS_REP = 65536;
constexpr u32 WS_KVF = 6u << 20;
constexpr u32 WS_PART = 1u << 20;
constexpr u32 WS_PQ = 2u << 20;
constexpr u32 WS_UT = 4u << 20;
constexpr u32 WS_DBG = 8u << 20;
constexpr u32 WS_BYTES = 8u << 20;
constexpr u64 EPOCH_INC = 0x9E3779B97F4A7C15ull;
constexpr u32 SPIN_CAP = 1u << 14;
#define CODE_BYTES 48000
#define D_RED 190
#define D_W 5
#define D_PQ0 330
#define D_PQ1 370
#define D_B3 320
#define H1POS 36
#define H2POS 118
#define H3POS 160
#define H4POS 200
#define TP3 50
#define M0 0x30
#define NOCLR 0
#define KW1 100
#define KW2 135
#define KW3 155
#define TORD 0x65218743u
#define HUP 6
#define ROT 0
#define HUB 6
#define WP 3
#define WPL 3
#define PACE 12
#define PLB 4
#define EBS 120
#define LPR 2
#define ZPOS 15
#define HRPOS 8
#define WO0 8
#define WOS 2
#define NWQ 4
#define NB1 18
#define WOP 8
#define NQB 6
#define QX 0
#define SORD 0x87654321u
#define MS0 0x0f
#define UPOS 1
#define QLATE 0
#define RSL 4
#define SSL 8
#define WPOS 3
#define D_KV 320
#define PHP 2
#define AXP 16
#define AXK 16
#define PR1 2
#define PR2 0
#define LAG 2
#define NPRE 33
#define KVS 41
#define PPW 40
#define NB0 16
#define NBG 4
#define D_H 20
#define PRS 0x01233
#define PG0 14
#define PG1 20
#define QW 41
#define QW2 48
#define C_RED 3750.f
#define C_PQ0 7200.f
#define C_PQ1 7050.f
#define A_PQ0 -30
#define A_PQ1 30
#define PRF 3
#define ORD 0
#define KO 1
#define WBC 0
#define WBC2 0u
#define WBP 10
#define WBW 55
#define XS 7
#define UH 3
#define RC0 2
#define PFT 1
#define KV0 8
#define KV1 8
#define KST 256
#define M8 0
#define PREP 1
#define PB3 1
#define KPR 0.f
#define DYN 1
#define SWP 1
#define FI 3
#define EXB 0
#define EXL -1
#define WOP0 16
#define SGN 0
#define NWP 4
#define EXP 0

constexpr int LDS_BYTES = 160 * 1024;
constexpr int JUNK = LDS_BYTES - 256;
constexpr int MISC = JUNK - 64;
constexpr int L_O = 448 * 224;
constexpr int RING = 6144, SLOT = 30720;
constexpr int L_WQ = L_O;
constexpr int L_TOP = RING + 5 * SLOT;
constexpr int L_FULL = L_TOP + 3072;
constexpr int KG_PITCH = 1040, KG_GRP = 56 * KG_PITCH;
constexpr int K_PITCH = 4352;
constexpr int A_K = MISC - 4 * K_PITCH;
constexpr int A_CZ = A_K - 64;
constexpr int A_PL = 0;
constexpr int U_PITCH = 4096 + 32;
constexpr int A_U = A_CZ - 2048 - 4 * U_PITCH;
static_assert(A_U % 16 == 0 && A_K % 16 == 0 && A_PL % 16 == 0 && A_U >= 2 * KG_GRP, "lds");
static_assert(L_WQ == RING + 4 * SLOT - 4 * 7168 && L_FULL + 512 <= MISC && L_O + 61440 <= L_TOP + 2048, "lds");
constexpr int slot_ix(int c) { return (0x0214302u >> (4 * c)) & 7; }
constexpr int slot_of(int c) { return RING + slot_ix(c) * SLOT; }

typedef _Float16 f16x2 __attribute__((ext_vector_type(2)));
typedef _Float16 f16x8 __attribute__((ext_vector_type(8)));
DEVINL float bf2f(u32 h) { return __uint_as_float(h << 16); }
DEVINL float bfhi(u32 w) { return __uint_as_float(w & 0xffff0000u); }
DEVINL u32 pk2(f32x2 v) { return __builtin_bit_cast(u32, __builtin_convertvector(v, bf16x2)); }
DEVINL u32 pk(float a, float b) { return pk2(f32x2{a, b}); }
DEVINL f32x4 mfma16(u32x4 a, u32x4 b, f32x4 c) {
  return __builtin_amdgcn_mfma_f32_16x16x32_bf16(__builtin_bit_cast(bf16x8, a), __builtin_bit_cast(bf16x8, b), c, 0, 0, 0);
}
DEVINL f32x16 mfma32(u32x4 a, u32x4 b, f32x16 c) {
  return __builtin_amdgcn_mfma_f32_32x32x16_bf16(__builtin_bit_cast(bf16x8, a), __builtin_bit_cast(bf16x8, b), c, 0, 0, 0);
}
DEVINL f32x16 mfma32h(u32x4 a, u32x4 b, f32x16 c) {
  return __builtin_amdgcn_mfma_f32_32x32x16_f16(__builtin_bit_cast(f16x8, a), __builtin_bit_cast(f16x8, b), c, 0, 0, 0);
}
DEVINL u32 pkh(float a, float b) { return __builtin_bit_cast(u32, __builtin_convertvector(f32x2{a, b}, f16x2)); }
DEVINL u32 hm1(u32 a, u32 w) { return __builtin_bit_cast(u32, __builtin_bit_cast(f16x2, a) * __builtin_bit_cast(f16x2, w).xx); }
template <bool CJ> DEVINL u32 hm2(u32 a_, u32 w_, u32 t) {
  const f16x2 a = __builtin_bit_cast(f16x2, a_), w = __builtin_bit_cast(f16x2, w_);
  return __builtin_bit_cast(u32, __builtin_elementwise_fma(CJ ? f16x2{a.y, -a.x} : f16x2{-a.y, a.x}, w.yy, __builtin_bit_cast(f16x2, t)));
}
DEVINL u32x2 trd(LAS char* p) {
  s16x4 v = __builtin_amdgcn_ds_read_tr16_b64_v4i16((LAS s16x4*)p);
  return __builtin_bit_cast(u32x2, v);
}
DEVINL u32x4 trd2(LAS char* p0, LAS char* p1) {
  u32x2 a = trd(p0), b = trd(p1);
  return u32x4{a.x, a.y, b.x, b.y};
}
typedef const GAS char* gptr;
DEVINL void dma16(gptr base, u32 off, u32 lds) {
  asm volatile("s_mov_b32 m0, %2\n\ts_nop 0\n\tglobal_load_lds_dwordx4 %0, %1" ::"v"(off), "s"(base), "s"(lds) : "memory");
}
DEVINL void dma16m(gptr base, u32 off, u32 lds) {
  asm volatile("s_mov_b32 m0, %2\n\ts_mov_b32 exec_hi, 0xffffff\n\tglobal_load_lds_dwordx4 %0, %1\n\ts_mov_b32 exec_hi, -1" ::"v"(off), "s"(base), "s"(lds) : "memory");
}
DEVINL void dma4n(gptr base, u32 off, u32 lds) {
  asm volatile("s_mov_b32 m0, %2\n\ts_nop 0\n\tglobal_load_lds_dword %0, %1 nt" ::"v"(off), "s"(base), "s"(lds) : "memory");
}
DEVINL void dma16n(gptr base, u32 off, u32 lds) {
  asm volatile("s_mov_b32 m0, %2\n\ts_nop 0\n\tglobal_load_lds_dwordx4 %0, %1 nt" ::"v"(off), "s"(base), "s"(lds) : "memory");
}
DEVINL void dma16c(gptr base, u32 off, u32 lds) {
  asm volatile("s_mov_b32 m0, %2\n\ts_nop 0\n\tglobal_load_lds_dwordx4 %0, %1 sc1" ::"v"(off), "s"(base), "s"(lds) : "memory");
}
DEVINL void dma_mark(u64 pc, u32 lds) {
  asm volatile("s_mov_b32 m0, %2\n\ts_mov_b64 exec, 1\n\tglobal_load_lds_dword %0, %1\n\ts_mov_b64 exec, -1" ::"v"(0u), "s"(pc), "s"(lds) : "memory");
}
template <int OFF> DEVINL void arrive_wait(u32 a, u32 f) {
  u32 n = 1u << 20;
  asm volatile(
      "v_cmp_eq_u32 vcc, 0, %0\n\ts_cbranch_vccz 2f\n"
      "1:\n\tds_read_b32 %0, %2 offset:%3\n\ts_sub_u32 %1, %1, 1\n\ts_waitcnt lgkmcnt(0)\n\t"
      "v_cmp_eq_u32 vcc, 0, %0\n\ts_cbranch_scc1 2f\n\ts_cbranch_vccnz 1b\n"
      "2:" : "+v"(f), "+s"(n) : "v"(a), "n"(OFF) : "memory", "scc", "vcc");
}
#define LSL 1
template <int OFF> DEVINL u32 idle_wait(u32 a) {
  u32 n = 1u << 20, f;
  asm volatile(
      "s_branch 3f\n"
      "1:\n\ts_sleep %4\n3:\n\tds_read_b32 %0, %2 offset:%3\n\ts_sub_u32 %1, %1, 1\n\ts_waitcnt lgkmcnt(0)\n\t"
      "v_cmp_eq_u32 vcc, 0, %0\n\ts_cbranch_scc1 2f\n\ts_cbranch_vccnz 1b\n"
      "2:" : "=&v"(f), "+s"(n) : "v"(a), "n"(OFF), "n"(LSL) : "memory", "scc", "vcc");
  return f;
}
DEVINL void touch(gptr ptr, u32 lds) {
  asm volatile("s_mov_b32 m0, %1\n\ts_nop 0\n\tglobal_load_lds_dword %0, off" ::"v"(ptr), "s"(lds) : "memory");
}
DEVINL void st16(gptr base, u32 off, u32x4 v) {
  asm volatile("global_store_dwordx4 %0, %1, %2 sc1\n\ts_nop 1" ::"v"(off), "v"(v), "s"(base) : "memory");
}
#define WAITV(n) asm volatile("s_waitcnt vmcnt(%0)" ::"n"(n) : "memory")
#define WOQ 8
#define BAR() do { asm volatile("s_waitcnt lgkmcnt(0)" ::: "memory"); __builtin_amdgcn_s_barrier(); asm volatile("" ::: "memory"); } while (0)
#define SCHED() __builtin_amdgcn_sched_barrier(0)
#define SGB(m, n) __builtin_amdgcn_sched_group_barrier((m), (n), 0)
#define RLX_AGENT __ATOMIC_RELAXED, __HIP_MEMORY_SCOPE_AGENT
#define RLX_WG __ATOMIC_RELAXED, __HIP_MEMORY_SCOPE_WORKGROUP
DEVINL u64 ld8(gptr p) { return __hip_atomic_load((GAS u64*)p, RLX_AGENT); }
DEVINL void st8(gptr p, u64 v) { __hip_atomic_store((GAS u64*)p, v, RLX_AGENT); }
DEVINL u32 now() { return (u32)__builtin_amdgcn_s_memrealtime(); }
DEVINL u32 rfl(u32 v) { return (u32)__builtin_amdgcn_readfirstlane(v); }
DEVINL u64 rfl64(u64 v) { return ((u64)rfl((u32)(v >> 32)) << 32) | rfl((u32)v); }
DEVINL void poll(gptr p, u64 T) {
#pragma unroll 1
  for (u32 spin = 0; spin < SPIN_CAP; spin++) {
    if (__all(ld8(p) == T)) break;
    __builtin_amdgcn_s_sleep(PLB);
  }
  asm volatile("" ::: "memory");
}
DEVINL u32x4 poll2(gptr p, u32x4 v, u32 lo) {
  if (__builtin_expect(!__all(v.x == lo && v.z == lo), 0)) {
#pragma unroll 1
    for (u32 spin = 0; spin < SPIN_CAP; spin++) {
      const u64 a = ld8(p), b = ld8(p + 8);
      v = u32x4{(u32)a, (u32)(a >> 32), (u32)b, (u32)(b >> 32)};
      if (__all(v.x == lo && v.z == lo)) break;
      __builtin_amdgcn_s_sleep(HUB);
    }
  }
  asm volatile("" ::: "memory");
  return v;
}
DEVINL void until(u32 t_) {
  u32 n = 255;
  asm volatile("" : "+v"(t_));
  const u32 t = rfl(t_);
  asm volatile(
      "s_branch 3f\n"
      "1:\n\ts_sleep 1\n3:\n\ts_memrealtime vcc\n\ts_waitcnt lgkmcnt(0)\n\ts_sub_u32 %0, %0, 1\n\ts_cbranch_scc1 2f\n\t"
      "s_sub_u32 vcc_lo, vcc_lo, %1\n\ts_cmp_lt_i32 vcc_lo, 0\n\ts_cbranch_scc1 1b\n"
      "2:" : "+s"(n) : "s"(t) : "memory", "scc", "vcc");
}
DEVINL u32 wave_max(u32 m) {
  m = max(m, (u32)__builtin_amdgcn_update_dpp(0, (int)m, 0x111, 0xf, 0xf, true));
  m = max(m, (u32)__builtin_amdgcn_update_dpp(0, (int)m, 0x112, 0xf, 0xf, true));
  m = max(m, (u32)__builtin_amdgcn_update_dpp(0, (int)m, 0x114, 0xf, 0xf, true));
  m = max(m, (u32)__builtin_amdgcn_update_dpp(0, (int)m, 0x118, 0xf, 0xf, true));
  return max(max((u32)__builtin_amdgcn_readlane(m, 15), (u32)__builtin_amdgcn_readlane(m, 31)),
             max((u32)__builtin_amdgcn_readlane(m, 47), (u32)__builtin_amdgcn_readlane(m, 63)));
}
DEVINL u32 max3(u32 a, u32 b, u32 c) { return max(max(a, b), c); }
DEVINL u32 maxq(u32 m, u32x4 v) { return max3(max3(m, v.x, v.y), v.z, v.w); }

DEVINL void st8v(gptr base, u32 off, u32x2 v) {
  asm volatile("global_store_dwordx2 %0, %1, %2 sc1\n\ts_nop 1" ::"v"(off), "v"(v), "s"(base) : "memory");
}
template <bool EPS> DEVINL float phie(float a) {
  constexpr float L = 1.44269504f, B = EPS ? 1.000001f : 1.f;
  return __builtin_amdgcn_fmed3f(__builtin_amdgcn_exp2f(__builtin_fmaf(a, L, -L * B)), 0.f, 1.f);
}
DEVINL u32 phim(float a0, float a1, float e0, float e1) {
  return __builtin_bit_cast(u32, __builtin_elementwise_max(__builtin_bit_cast(s16x2, pk(a0, a1)), __builtin_bit_cast(s16x2, pk(e0, e1))));
}
template <bool EPS> DEVINL u32 phi2(float a0, float a1) {
  const float e0 = phie<EPS>(a0), e1 = phie<EPS>(a1);
  return EPS ? phim(a0, a1, e0 + 1e-6f, e1 + 1e-6f) : phim(a0, a1, e0, e1);
}
template <int OFF> DEVINL void put_masked(u32 a, f32x4 v, u64 mask) {
  asm volatile("s_mov_b64 exec, %2\n\tds_write_b128 %0, %1 offset:%3\n\ts_mov_b64 exec, -1" ::"v"(a), "v"(v), "s"(mask), "n"(OFF) : "memory");
}
template <class A> DEVINL void pin(A& a) { asm volatile("" : "+v"(a)); }
template <class A, class B> DEVINL void pin2(A& a, B b) { asm volatile("" : "+v"(a) : "v"(b)); }
DEVINL void set_m0(u32 v) { asm volatile("s_mov_b32 m0, %0\n\ts_nop 0" ::"s"(v) : "memory"); }
DEVINL float shr1(float v) { return __builtin_bit_cast(float, __builtin_amdgcn_update_dpp(0, __builtin_bit_cast(int, v), 0x138, 0xf, 0xf, true)); }
DEVINL u32x2 pk4(f32x4 a) { return u32x2{pk(a[0], a[1]), pk(a[2], a[3])}; }
DEVINL u32x4 cat(u32x2 a, u32x2 b) { return u32x4{a.x, a.y, b.x, b.y}; }
DEVINL u32 bline(u32 c, u32 lane, u32 jj, u32 xc) {
  const u32 col = (xc & 1) * 384u + (jj & 3u) * 128u;
  const u32 sq = ((c - 7u) * 256u + (jj >> 2)) * 2688u + col;
  const u32 m0 = c < 7u ? 14336u : 10752u, s0 = c < 7u ? ((xc >> 1) * 512u + jj) * 896u + c * 128u : sq;
  const u32 m1 = c < 7u ? 5376u : 10752u, s1 = c < 7u ? (c * 64u - 64u + (jj >> 3)) * 2688u + 896u + (jj & 4u) * 224u + col : sq;
  const u32 l = c == 8u ? min(lane, 47u) : lane;
  return lane < 32u ? l * m0 + s0 : l * m1 + s1;
}

template <int I> struct IC { static constexpr int v = I; };
template <int B, int E, class F> DEVINL void static_for(F&& f) {
  if constexpr (B < E) { f(IC<B>{}); static_for<B + 1, E>(f); }
}
#define CONV_ASM \
  "s_setprio 3\n" \
  "s_mov_b32 m0, %[pw]\n" \
  "ds_read_b64_tr_b16 v[128:129], %[kb] offset:32\n" \
  "ds_read_b64_tr_b16 v[130:131], %[kb] offset:0\n" \
  "ds_read_b64_tr_b16 v[64:65], %[ub] offset:0\n" \
  "ds_read_b64_tr_b16 v[66:67], %[ub] offset:32\n" \
  "ds_read_b64_tr_b16 v[68:69], %[ub] offset:256\n" \
  "ds_read_b64_tr_b16 v[70:71], %[ub] offset:288\n" \
  "ds_read_b64_tr_b16 v[72:73], %[ub] offset:512\n" \
  "ds_read_b64_tr_b16 v[74:75], %[ub] offset:544\n" \
  "ds_read_b64_tr_b16 v[132:133], %[kb] offset:288\n" \
  "ds_read_b64_tr_b16 v[134:135], %[kb] offset:256\n" \
  "ds_read_b64_tr_b16 v[76:77], %[ub] offset:768\n" \
  "ds_read_b64_tr_b16 v[78:79], %[ub] offset:800\n" \
  "ds_read_b64_tr_b16 v[136:137], %[kb] offset:544\n" \
  "ds_read_b64_tr_b16 v[138:139], %[kb] offset:512\n" \
  "s_waitcnt lgkmcnt(10)\n" \
  "v_mfma_f32_16x16x32_bf16 v[192:195], v[128:131], v[64:67], 0\n" \
  "ds_read_b64_tr_b16 v[80:81], %[ub] offset:1024\n" \
  "ds_read_b64_tr_b16 v[82:83], %[ub] offset:1056\n" \
  "s_waitcnt lgkmcnt(10)\n" \
  "v_mfma_f32_16x16x32_bf16 v[196:199], v[128:131], v[68:71], 0\n" \
  "ds_read_b64_tr_b16 v[140:141], %[kb] offset:800\n" \
  "ds_read_b64_tr_b16 v[142:143], %[kb] offset:768\n" \
  "s_waitcnt lgkmcnt(10)\n" \
  "v_mfma_f32_16x16x32_bf16 v[200:203], v[128:131], v[72:75], 0\n" \
  "s_waitcnt lgkmcnt(8)\n" \
  "v_mfma_f32_16x16x32_bf16 v[196:199], v[132:135], v[64:67], v[196:199]\n" \
  "ds_read_b64_tr_b16 v[84:85], %[ub] offset:1280\n" \
  "ds_read_b64_tr_b16 v[86:87], %[ub] offset:1312\n" \
  "v_mfma_f32_16x16x32_bf16 v[200:203], v[132:135], v[68:71], v[200:203]\n" \
  "s_waitcnt lgkmcnt(8)\n" \
  "v_mfma_f32_16x16x32_bf16 v[204:207], v[128:131], v[76:79], 0\n" \
  "s_waitcnt lgkmcnt(6)\n" \
  "v_mfma_f32_16x16x32_bf16 v[200:203], v[136:139], v[64:67], v[200:203]\n" \
  "ds_read_b64_tr_b16 v[144:145], %[kb] offset:1056\n" \
  "ds_read_b64_tr_b16 v[146:147], %[kb] offset:1024\n" \
  "v_add_f32_dpp v194, v195, v194 wave_shr:1 row_mask:0xf bank_mask:0xf bound_ctrl:1\n" \
  "v_mfma_f32_16x16x32_bf16 v[204:207], v[132:135], v[72:75], v[204:207]\n" \
  "s_nop 0\n" \
  "v_add_f32_dpp v193, v194, v193 wave_shr:1 row_mask:0xf bank_mask:0xf bound_ctrl:1\n" \
  "s_waitcnt lgkmcnt(6)\n" \
  "v_mfma_f32_16x16x32_bf16 v[208:211], v[128:131], v[80:83], 0\n" \
  "ds_read_b64_tr_b16 v[88:89], %[ub] offset:1536\n" \
  "ds_read_b64_tr_b16 v[90:91], %[ub] offset:1568\n" \
  "v_add_f32_dpp v192, v193, v192 wave_shr:1 row_mask:0xf bank_mask:0xf bound_ctrl:1\n" \
  "v_mfma_f32_16x16x32_bf16 v[204:207], v[136:139], v[68:71], v[204:207]\n" \
  "v_mov_b32_dpp v193, v194 row_shl:4 row_mask:0x8 bank_mask:0x4\n" \
  "v_mov_b32_dpp v193, v195 row_shl:8 row_mask:0x8 bank_mask:0x2\n" \
  "v_mfma_f32_16x16x32_bf16 v[208:211], v[132:135], v[76:79], v[208:211]\n" \
  "ds_write_addtid_b32 v192 offset:0\n" \
  "s_mov_b64 exec, %[l63]\n" \
  "ds_write_b32 %[cw], v193 offset:1920\n" \
  "s_mov_b64 exec, -1\n" \
  "s_waitcnt lgkmcnt(8)\n" \
  "v_mfma_f32_16x16x32_bf16 v[204:207], v[140:143], v[64:67], v[204:207]\n" \
  "v_add_f32_dpp v198, v199, v198 wave_shr:1 row_mask:0xf bank_mask:0xf bound_ctrl:1\n" \
  "v_mfma_f32_16x16x32_bf16 v[208:211], v[136:139], v[72:75], v[208:211]\n" \
  "s_nop 0\n" \
  "v_add_f32_dpp v197, v198, v197 wave_shr:1 row_mask:0xf bank_mask:0xf bound_ctrl:1\n" \
  "s_waitcnt lgkmcnt(6)\n" \
  "v_mfma_f32_16x16x32_bf16 v[212:215], v[128:131], v[84:87], 0\n" \
  "ds_read_b64_tr_b16 v[148:149], %[kb] offset:1312\n" \
  "ds_read_b64_tr_b16 v[150:151], %[kb] offset:1280\n" \
  "v_add_f32_dpp v196, v197, v196 wave_shr:1 row_mask:0xf bank_mask:0xf bound_ctrl:1\n" \
  "v_mfma_f32_16x16x32_bf16 v[208:211], v[140:143], v[68:71], v[208:211]\n" \
  "v_mov_b32_dpp v197, v198 row_shl:4 row_mask:0x8 bank_mask:0x4\n" \
  "v_mov_b32_dpp v197, v199 row_shl:8 row_mask:0x8 bank_mask:0x2\n" \
  "s_setprio 2\n" \
  "v_mfma_f32_16x16x32_bf16 v[212:215], v[132:135], v[80:83], v[212:215]\n" \
  "ds_read_b64_tr_b16 v[92:93], %[ub] offset:1792\n" \
  "ds_read_b64_tr_b16 v[94:95], %[ub] offset:1824\n" \
  "ds_write_addtid_b32 v196 offset:512\n" \
  "s_mov_b64 exec, %[l63]\n" \
  "ds_write_b32 %[cw], v197 offset:1792\n" \
  "s_mov_b64 exec, -1\n" \
  "s_waitcnt lgkmcnt(10)\n" \
  "v_mfma_f32_16x16x32_bf16 v[208:211], v[144:147], v[64:67], v[208:211]\n" \
  "v_add_f32_dpp v202, v203, v202 wave_shr:1 row_mask:0xf bank_mask:0xf bound_ctrl:1\n" \
  "v_mfma_f32_16x16x32_bf16 v[212:215], v[136:139], v[76:79], v[212:215]\n" \
  "s_nop 0\n" \
  "v_add_f32_dpp v201, v202, v201 wave_shr:1 row_mask:0xf bank_mask:0xf bound_ctrl:1\n" \
  "s_waitcnt lgkmcnt(8)\n" \
  "v_mfma_f32_16x16x32_bf16 v[216:219], v[128:131], v[88:91], 0\n" \
  "v_add_f32_dpp v200, v201, v200 wave_shr:1 row_mask:0xf bank_mask:0xf bound_ctrl:1\n" \
  "v_mfma_f32_16x16x32_bf16 v[212:215], v[140:143], v[72:75], v[212:215]\n" \
  "v_mov_b32_dpp v201, v202 row_shl:4 row_mask:0x8 bank_mask:0x4\n" \
  "v_mov_b32_dpp v201, v203 row_shl:8 row_mask:0x8 bank_mask:0x2\n" \
  "v_mfma_f32_16x16x32_bf16 v[216:219], v[132:135], v[84:87], v[216:219]\n" \
  "ds_read_b64_tr_b16 v[152:153], %[kb] offset:1568\n" \
  "ds_read_b64_tr_b16 v[154:155], %[kb] offset:1536\n" \
  "ds_write_addtid_b32 v200 offset:1024\n" \
  "s_mov_b64 exec, %[l63]\n" \
  "ds_write_b32 %[cw], v201 offset:1664\n" \
  "s_mov_b64 exec, -1\n" \
  "v_mfma_f32_16x16x32_bf16 v[212:215], v[144:147], v[68:71], v[212:215]\n" \
  "v_add_f32_dpp v206, v207, v206 wave_shr:1 row_mask:0xf bank_mask:0xf bound_ctrl:1\n" \
  "v_mfma_f32_16x16x32_bf16 v[216:219], v[136:139], v[80:83], v[216:219]\n" \
  "ds_read_b64_tr_b16 v[96:97], %[ub] offset:2048\n" \
  "ds_read_b64_tr_b16 v[98:99], %[ub] offset:2080\n" \
  "v_add_f32_dpp v205, v206, v205 wave_shr:1 row_mask:0xf bank_mask:0xf bound_ctrl:1\n" \
  "s_waitcnt lgkmcnt(10)\n" \
  "v_mfma_f32_16x16x32_bf16 v[212:215], v[148:151], v[64:67], v[212:215]\n" \
  "v_add_f32_dpp v204, v205, v204 wave_shr:1 row_mask:0xf bank_mask:0xf bound_ctrl:1\n" \
  "s_setprio 1\n" \
  "v_mfma_f32_16x16x32_bf16 v[216:219], v[140:143], v[76:79], v[216:219]\n" \
  "v_mov_b32_dpp v205, v206 row_shl:4 row_mask:0x8 bank_mask:0x4\n" \
  "v_mov_b32_dpp v205, v207 row_shl:8 row_mask:0x8 bank_mask:0x2\n" \
  "s_waitcnt lgkmcnt(8)\n" \
  "v_mfma_f32_16x16x32_bf16 v[220:223], v[128:131], v[92:95], 0\n" \
  "ds_write_addtid_b32 v204 offset:1536\n" \
  "s_mov_b64 exec, %[l63]\n" \
  "ds_write_b32 %[cw], v205 offset:1536\n" \
  "s_mov_b64 exec, -1\n" \
  "v_mfma_f32_16x16x32_bf16 v[216:219], v[144:147], v[72:75], v[216:219]\n" \
  "v_add_f32_dpp v210, v211, v210 wave_shr:1 row_mask:0xf bank_mask:0xf bound_ctrl:1\n" \
  "v_mfma_f32_16x16x32_bf16 v[220:223], v[132:135], v[88:91], v[220:223]\n" \
  "s_nop 0\n" \
  "v_add_f32_dpp v209, v210, v209 wave_shr:1 row_mask:0xf bank_mask:0xf bound_ctrl:1\n" \
  "v_mfma_f32_16x16x32_bf16 v[216:219], v[148:151], v[68:71], v[216:219]\n" \
  "s_nop 0\n" \
  "v_add_f32_dpp v208, v209, v208 wave_shr:1 row_mask:0xf bank_mask:0xf bound_ctrl:1\n" \
  "v_mfma_f32_16x16x32_bf16 v[220:223], v[136:139], v[84:87], v[220:223]\n" \
  "ds_read_b64_tr_b16 v[156:157], %[kb] offset:1824\n" \
  "ds_read_b64_tr_b16 v[158:159], %[kb] offset:1792\n" \
  "v_mov_b32_dpp v209, v210 row_shl:4 row_mask:0x8 bank_mask:0x4\n" \
  "v_mov_b32_dpp v209, v211 row_shl:8 row_mask:0x8 bank_mask:0x2\n" \
  "s_setprio 0\n" \
  "s_waitcnt lgkmcnt(8)\n" \
  "v_mfma_f32_16x16x32_bf16 v[216:219], v[152:155], v[64:67], v[216:219]\n" \
  "ds_write_addtid_b32 v208 offset:2048\n" \
  "s_mov_b64 exec, %[l63]\n" \
  "ds_write_b32 %[cw], v209 offset:1408\n" \
  "s_mov_b64 exec, -1\n" \
  "v_mfma_f32_16x16x32_bf16 v[220:223], v[140:143], v[80:83], v[220:223]\n" \
  "ds_read_b64_tr_b16 v[100:101], %[ub] offset:2304\n" \
  "ds_read_b64_tr_b16 v[102:103], %[ub] offset:2336\n" \
  "v_add_f32_dpp v214, v215, v214 wave_shr:1 row_mask:0xf bank_mask:0xf bound_ctrl:1\n" \
  "s_waitcnt lgkmcnt(8)\n" \
  "v_mfma_f32_16x16x32_bf16 v[192:195], v[128:131], v[96:99], 0\n" \
  "v_add_f32_dpp v213, v214, v213 wave_shr:1 row_mask:0xf bank_mask:0xf bound_ctrl:1\n" \
  "s_waitcnt lgkmcnt(0)\n" \
  "s_barrier\n" \
  "s_setprio 3\n" \
  "ds_read2st64_b32 v[224:225], %[prd] offset0:0 offset1:32\n" \
  "ds_read2st64_b32 v[226:227], %[prd] offset0:64 offset1:96\n" \
  "ds_read_b128 v[228:231], %[crd]\n" \
  "v_mfma_f32_16x16x32_bf16 v[220:223], v[144:147], v[76:79], v[220:223]\n" \
  "v_add_f32_dpp v212, v213, v212 wave_shr:1 row_mask:0xf bank_mask:0xf bound_ctrl:1\n" \
  "v_mfma_f32_16x16x32_bf16 v[192:195], v[132:135], v[92:95], v[192:195]\n" \
  "v_mov_b32_dpp v213, v214 row_shl:4 row_mask:0x8 bank_mask:0x4\n" \
  "v_mov_b32_dpp v213, v215 row_shl:8 row_mask:0x8 bank_mask:0x2\n" \
  "v_mfma_f32_16x16x32_bf16 v[220:223], v[148:151], v[72:75], v[220:223]\n" \
  "ds_write_addtid_b32 v212 offset:2560\n" \
  "s_mov_b64 exec, %[l63]\n" \
  "ds_write_b32 %[cw], v213 offset:1280\n" \
  "s_mov_b64 exec, -1\n" \
  "v_mfma_f32_16x16x32_bf16 v[192:195], v[136:139], v[88:91], v[192:195]\n" \
  "v_add_f32_dpp v218, v219, v218 wave_shr:1 row_mask:0xf bank_mask:0xf bound_ctrl:1\n" \
  "v_mfma_f32_16x16x32_bf16 v[220:223], v[152:155], v[68:71], v[220:223]\n" \
  "s_nop 0\n" \
  "v_add_f32_dpp v217, v218, v217 wave_shr:1 row_mask:0xf bank_mask:0xf bound_ctrl:1\n" \
  "v_mfma_f32_16x16x32_bf16 v[192:195], v[140:143], v[84:87], v[192:195]\n" \
  "ds_read_b64_tr_b16 v[160:161], %[kb] offset:2080\n" \
  "ds_read_b64_tr_b16 v[162:163], %[kb] offset:2048\n" \
  "v_add_f32_dpp v216, v217, v216 wave_shr:1 row_mask:0xf bank_mask:0xf bound_ctrl:1\n" \
  "s_waitcnt lgkmcnt(4)\n" \
  "v_add_f32 v224, v224, v228\n" \
  "v_add_f32 v225, v225, v229\n" \
  "v_mfma_f32_16x16x32_bf16 v[220:223], v[156:159], v[64:67], v[220:223]\n" \
  "v_mov_b32_dpp v217, v218 row_shl:4 row_mask:0x8 bank_mask:0x4\n" \
  "v_mov_b32_dpp v217, v219 row_shl:8 row_mask:0x8 bank_mask:0x2\n" \
  "v_add_f32 v226, v226, v230\n" \
  "v_add_f32 v227, v227, v231\n" \
  "v_mfma_f32_16x16x32_bf16 v[192:195], v[144:147], v[80:83], v[192:195]\n" \
  "ds_read_b64_tr_b16 v[104:105], %[ub] offset:2560\n" \
  "ds_read_b64_tr_b16 v[106:107], %[ub] offset:2592\n" \
  "ds_write_addtid_b32 v216 offset:3072\n" \
  "s_mov_b64 exec, %[l63]\n" \
  "ds_write_b32 %[cw], v217 offset:1152\n" \
  "s_mov_b64 exec, -1\n" \
  "v_cvt_pk_bf16_f32 v232, v224, v225\n" \
  "v_cvt_pk_bf16_f32 v233, v226, v227\n" \
  "v_mfma_f32_16x16x32_bf16 v[196:199], v[128:131], v[100:103], 0\n" \
  "global_store_dwordx2 %[yo], v[232:233], %[o0]\n" \
  "v_sub_u32 %[crd], %[crd], %[cst]\n" \
  "v_mfma_f32_16x16x32_bf16 v[192:195], v[148:151], v[76:79], v[192:195]\n" \
  "v_mfma_f32_16x16x32_bf16 v[196:199], v[132:135], v[96:99], v[196:199]\n" \
  "v_mfma_f32_16x16x32_bf16 v[192:195], v[152:155], v[72:75], v[192:195]\n" \
  "s_setprio 2\n" \
  "v_mfma_f32_16x16x32_bf16 v[196:199], v[136:139], v[92:95], v[196:199]\n" \
  "v_add_f32_dpp v222, v223, v222 wave_shr:1 row_mask:0xf bank_mask:0xf bound_ctrl:1\n" \
  "v_mfma_f32_16x16x32_bf16 v[192:195], v[156:159], v[68:71], v[192:195]\n" \
  "s_nop 0\n" \
  "v_add_f32_dpp v221, v222, v221 wave_shr:1 row_mask:0xf bank_mask:0xf bound_ctrl:1\n" \
  "v_mfma_f32_16x16x32_bf16 v[196:199], v[140:143], v[88:91], v[196:199]\n" \
  "s_nop 0\n" \
  "v_add_f32_dpp v220, v221, v220 wave_shr:1 row_mask:0xf bank_mask:0xf bound_ctrl:1\n" \
  "s_waitcnt lgkmcnt(4)\n" \
  "v_mfma_f32_16x16x32_bf16 v[192:195], v[160:163], v[64:67], v[192:195]\n" \
  "v_mov_b32_dpp v221, v222 row_shl:4 row_mask:0x8 bank_mask:0x4\n" \
  "v_mov_b32_dpp v221, v223 row_shl:8 row_mask:0x8 bank_mask:0x2\n" \
  "v_mfma_f32_16x16x32_bf16 v[196:199], v[144:147], v[84:87], v[196:199]\n" \
  "ds_read_b64_tr_b16 v[164:165], %[kb] offset:2336\n" \
  "ds_read_b64_tr_b16 v[166:167], %[kb] offset:2304\n" \
  "ds_write_addtid_b32 v220 offset:3584\n" \
  "s_mov_b64 exec, %[l63]\n" \
  "ds_write_b32 %[cw], v221 offset:1024\n" \
  "s_mov_b64 exec, -1\n" \
  "s_waitcnt lgkmcnt(6)\n" \
  "v_mfma_f32_16x16x32_bf16 v[200:203], v[128:131], v[104:107], 0\n" \
  "v_mfma_f32_16x16x32_bf16 v[196:199], v[148:151], v[80:83], v[196:199]\n" \
  "ds_read_b64_tr_b16 v[108:109], %[ub] offset:2816\n" \
  "ds_read_b64_tr_b16 v[110:111], %[ub] offset:2848\n" \
  "v_mfma_f32_16x16x32_bf16 v[200:203], v[132:135], v[100:103], v[200:203]\n" \
  "v_mfma_f32_16x16x32_bf16 v[196:199], v[152:155], v[76:79], v[196:199]\n" \
  "s_setprio 1\n" \
  "v_mfma_f32_16x16x32_bf16 v[200:203], v[136:139], v[96:99], v[200:203]\n" \
  "v_add_f32_dpp v194, v195, v194 wave_shr:1 row_mask:0xf bank_mask:0xf bound_ctrl:1\n" \
  "v_mfma_f32_16x16x32_bf16 v[196:199], v[156:159], v[72:75], v[196:199]\n" \
  "s_nop 0\n" \
  "v_add_f32_dpp v193, v194, v193 wave_shr:1 row_mask:0xf bank_mask:0xf bound_ctrl:1\n" \
  "v_mfma_f32_16x16x32_bf16 v[200:203], v[140:143], v[92:95], v[200:203]\n" \
  "s_nop 0\n" \
  "v_add_f32_dpp v192, v193, v192 wave_shr:1 row_mask:0xf bank_mask:0xf bound_ctrl:1\n" \
  "v_mfma_f32_16x16x32_bf16 v[196:199], v[160:163], v[68:71], v[196:199]\n" \
  "v_mov_b32_dpp v193, v194 row_shl:4 row_mask:0x8 bank_mask:0x4\n" \
  "v_mov_b32_dpp v193, v195 row_shl:8 row_mask:0x8 bank_mask:0x2\n" \
  "v_mfma_f32_16x16x32_bf16 v[200:203], v[144:147], v[88:91], v[200:203]\n" \
  "ds_write_addtid_b32 v192 offset:4096\n" \
  "s_mov_b64 exec, %[l63]\n" \
  "ds_write_b32 %[cw], v193 offset:896\n" \
  "s_mov_b64 exec, -1\n" \
  "s_waitcnt lgkmcnt(6)\n" \
  "v_mfma_f32_16x16x32_bf16 v[196:199], v[164:167], v[64:67], v[196:199]\n" \
  "s_setprio 0\n" \
  "v_mfma_f32_16x16x32_bf16 v[200:203], v[148:151], v[84:87], v[200:203]\n" \
  "ds_read_b64_tr_b16 v[168:169], %[kb] offset:2592\n" \
  "ds_read_b64_tr_b16 v[170:171], %[kb] offset:2560\n" \
  "s_waitcnt lgkmcnt(4)\n" \
  "v_mfma_f32_16x16x32_bf16 v[204:207], v[128:131], v[108:111], 0\n" \
  "v_mfma_f32_16x16x32_bf16 v[200:203], v[152:155], v[80:83], v[200:203]\n" \
  "ds_read_b64_tr_b16 v[112:113], %[ub] offset:3072\n" \
  "ds_read_b64_tr_b16 v[114:115], %[ub] offset:3104\n" \
  "s_waitcnt lgkmcnt(0)\n" \
  "s_barrier\n" \
  "s_setprio 3\n" \
  "ds_read2st64_b32 v[224:225], %[prd] offset0:8 offset1:40\n" \
  "ds_read2st64_b32 v[226:227], %[prd] offset0:72 offset1:104\n" \
  "ds_read_b128 v[228:231], %[crd]\n" \
  "v_mfma_f32_16x16x32_bf16 v[204:207], v[132:135], v[104:107], v[204:207]\n" \
  "v_mfma_f32_16x16x32_bf16 v[200:203], v[156:159], v[76:79], v[200:203]\n" \
  "v_mfma_f32_16x16x32_bf16 v[204:207], v[136:139], v[100:103], v[204:207]\n" \
  "v_add_f32_dpp v198, v199, v198 wave_shr:1 row_mask:0xf bank_mask:0xf bound_ctrl:1\n" \
  "v_mfma_f32_16x16x32_bf16 v[200:203], v[160:163], v[72:75], v[200:203]\n" \
  "s_nop 0\n" \
  "v_add_f32_dpp v197, v198, v197 wave_shr:1 row_mask:0xf bank_mask:0xf bound_ctrl:1\n" \
  "v_mfma_f32_16x16x32_bf16 v[204:207], v[140:143], v[96:99], v[204:207]\n" \
  "s_nop 0\n" \
  "v_add_f32_dpp v196, v197, v196 wave_shr:1 row_mask:0xf bank_mask:0xf bound_ctrl:1\n" \
  "v_mfma_f32_16x16x32_bf16 v[200:203], v[164:167], v[68:71], v[200:203]\n" \
  "v_mov_b32_dpp v197, v198 row_shl:4 row_mask:0x8 bank_mask:0x4\n" \
  "v_mov_b32_dpp v197, v199 row_shl:8 row_mask:0x8 bank_mask:0x2\n" \
  "s_waitcnt lgkmcnt(0)\n" \
  "v_add_f32 v224, v224, v228\n" \
  "v_add_f32 v225, v225, v229\n" \
  "v_mfma_f32_16x16x32_bf16 v[204:207], v[144:147], v[92:95], v[204:207]\n" \
  "ds_write_addtid_b32 v196 offset:4608\n" \
  "s_mov_b64 exec, %[l63]\n" \
  "ds_write_b32 %[cw], v197 offset:768\n" \
  "s_mov_b64 exec, -1\n" \
  "v_add_f32 v226, v226, v230\n" \
  "v_add_f32 v227, v227, v231\n" \
  "v_mfma_f32_16x16x32_bf16 v[200:203], v[168:171], v[64:67], v[200:203]\n" \
  "v_cvt_pk_bf16_f32 v232, v224, v225\n" \
  "v_cvt_pk_bf16_f32 v233, v226, v227\n" \
  "v_mfma_f32_16x16x32_bf16 v[204:207], v[148:151], v[88:91], v[204:207]\n" \
  "global_store_dwordx2 %[yo], v[232:233], %[o1]\n" \
  "v_sub_u32 %[crd], %[crd], %[cst]\n" \
  "v_mfma_f32_16x16x32_bf16 v[208:211], v[128:131], v[112:115], 0\n" \
  "v_mfma_f32_16x16x32_bf16 v[204:207], v[152:155], v[84:87], v[204:207]\n" \
  "ds_read_b64_tr_b16 v[172:173], %[kb] offset:2848\n" \
  "ds_read_b64_tr_b16 v[174:175], %[kb] offset:2816\n" \
  "v_mfma_f32_16x16x32_bf16 v[208:211], v[132:135], v[108:111], v[208:211]\n" \
  "v_mfma_f32_16x16x32_bf16 v[204:207], v[156:159], v[80:83], v[204:207]\n" \
  "ds_read_b64_tr_b16 v[116:117], %[ub] offset:3328\n" \
  "ds_read_b64_tr_b16 v[118:119], %[ub] offset:3360\n" \
  "v_mfma_f32_16x16x32_bf16 v[208:211], v[136:139], v[104:107], v[208:211]\n" \
  "v_add_f32_dpp v202, v203, v202 wave_shr:1 row_mask:0xf bank_mask:0xf bound_ctrl:1\n" \
  "v_mfma_f32_16x16x32_bf16 v[204:207], v[160:163], v[76:79], v[204:207]\n" \
  "s_nop 0\n" \
  "v_add_f32_dpp v201, v202, v201 wave_shr:1 row_mask:0xf bank_mask:0xf bound_ctrl:1\n" \
  "s_setprio 2\n" \
  "v_mfma_f32_16x16x32_bf16 v[208:211], v[140:143], v[100:103], v[208:211]\n" \
  "v_add_f32_dpp v200, v201, v200 wave_shr:1 row_mask:0xf bank_mask:0xf bound_ctrl:1\n" \
  "v_mfma_f32_16x16x32_bf16 v[204:207], v[164:167], v[72:75], v[204:207]\n" \
  "v_mov_b32_dpp v201, v202 row_shl:4 row_mask:0x8 bank_mask:0x4\n" \
  "v_mov_b32_dpp v201, v203 row_shl:8 row_mask:0x8 bank_mask:0x2\n" \
  "v_mfma_f32_16x16x32_bf16 v[208:211], v[144:147], v[96:99], v[208:211]\n" \
  "ds_write_addtid_b32 v200 offset:5120\n" \
  "s_mov_b64 exec, %[l63]\n" \
  "ds_write_b32 %[cw], v201 offset:640\n" \
  "s_mov_b64 exec, -1\n" \
  "v_mfma_f32_16x16x32_bf16 v[204:207], v[168:171], v[68:71], v[204:207]\n" \
  "v_mfma_f32_16x16x32_bf16 v[208:211], v[148:151], v[92:95], v[208:211]\n" \
  "s_waitcnt lgkmcnt(4)\n" \
  "v_mfma_f32_16x16x32_bf16 v[204:207], v[172:175], v[64:67], v[204:207]\n" \
  "v_mfma_f32_16x16x32_bf16 v[208:211], v[152:155], v[88:91], v[208:211]\n" \
  "s_waitcnt lgkmcnt(2)\n" \
  "v_mfma_f32_16x16x32_bf16 v[212:215], v[128:131], v[116:119], 0\n" \
  "v_mfma_f32_16x16x32_bf16 v[208:211], v[156:159], v[84:87], v[208:211]\n" \
  "ds_read_b64_tr_b16 v[176:177], %[kb] offset:3104\n" \
  "ds_read_b64_tr_b16 v[178:179], %[kb] offset:3072\n" \
  "s_setprio 1\n" \
  "v_mfma_f32_16x16x32_bf16 v[212:215], v[132:135], v[112:115], v[212:215]\n" \
  "v_mfma_f32_16x16x32_bf16 v[208:211], v[160:163], v[80:83], v[208:211]\n" \
  "ds_read_b64_tr_b16 v[120:121], %[ub] offset:3584\n" \
  "ds_read_b64_tr_b16 v[122:123], %[ub] offset:3616\n" \
  "v_mfma_f32_16x16x32_bf16 v[212:215], v[136:139], v[108:111], v[212:215]\n" \
  "v_add_f32_dpp v206, v207, v206 wave_shr:1 row_mask:0xf bank_mask:0xf bound_ctrl:1\n" \
  "v_mfma_f32_16x16x32_bf16 v[208:211], v[164:167], v[76:79], v[208:211]\n" \
  "s_nop 0\n" \
  "v_add_f32_dpp v205, v206, v205 wave_shr:1 row_mask:0xf bank_mask:0xf bound_ctrl:1\n" \
  "v_mfma_f32_16x16x32_bf16 v[212:215], v[140:143], v[104:107], v[212:215]\n" \
  "s_nop 0\n" \
  "v_add_f32_dpp v204, v205, v204 wave_shr:1 row_mask:0xf bank_mask:0xf bound_ctrl:1\n" \
  "v_mfma_f32_16x16x32_bf16 v[208:211], v[168:171], v[72:75], v[208:211]\n" \
  "v_mov_b32_dpp v205, v206 row_shl:4 row_mask:0x8 bank_mask:0x4\n" \
  "v_mov_b32_dpp v205, v207 row_shl:8 row_mask:0x8 bank_mask:0x2\n" \
  "s_setprio 0\n" \
  "v_mfma_f32_16x16x32_bf16 v[212:215], v[144:147], v[100:103], v[212:215]\n" \
  "ds_write_addtid_b32 v204 offset:5632\n" \
  "s_mov_b64 exec, %[l63]\n" \
  "ds_write_b32 %[cw], v205 offset:512\n" \
  "s_mov_b64 exec, -1\n" \
  "v_mfma_f32_16x16x32_bf16 v[208:211], v[172:175], v[68:71], v[208:211]\n" \
  "v_mfma_f32_16x16x32_bf16 v[212:215], v[148:151], v[96:99], v[212:215]\n" \
  "s_waitcnt lgkmcnt(0)\n" \
  "s_barrier\n" \
  "s_setprio 3\n" \
  "ds_read2st64_b32 v[224:225], %[prd] offset0:16 offset1:48\n" \
  "ds_read2st64_b32 v[226:227], %[prd] offset0:80 offset1:112\n" \
  "ds_read_b128 v[228:231], %[crd]\n" \
  "v_mfma_f32_16x16x32_bf16 v[208:211], v[176:179], v[64:67], v[208:211]\n" \
  "v_mfma_f32_16x16x32_bf16 v[212:215], v[152:155], v[92:95], v[212:215]\n" \
  "v_mfma_f32_16x16x32_bf16 v[216:219], v[128:131], v[120:123], 0\n" \
  "v_mfma_f32_16x16x32_bf16 v[212:215], v[156:159], v[88:91], v[212:215]\n" \
  "v_mfma_f32_16x16x32_bf16 v[216:219], v[132:135], v[116:119], v[216:219]\n" \
  "v_mfma_f32_16x16x32_bf16 v[212:215], v[160:163], v[84:87], v[212:215]\n" \
  "ds_read_b64_tr_b16 v[180:181], %[kb] offset:3360\n" \
  "ds_read_b64_tr_b16 v[182:183], %[kb] offset:3328\n" \
  "s_waitcnt lgkmcnt(2)\n" \
  "v_add_f32 v224, v224, v228\n" \
  "v_add_f32 v225, v225, v229\n" \
  "v_mfma_f32_16x16x32_bf16 v[216:219], v[136:139], v[112:115], v[216:219]\n" \
  "v_add_f32_dpp v210, v211, v210 wave_shr:1 row_mask:0xf bank_mask:0xf bound_ctrl:1\n" \
  "v_add_f32 v226, v226, v230\n" \
  "v_add_f32 v227, v227, v231\n" \
  "v_mfma_f32_16x16x32_bf16 v[212:215], v[164:167], v[80:83], v[212:215]\n" \
  "ds_read_b64_tr_b16 v[124:125], %[ub] offset:3840\n" \
  "ds_read_b64_tr_b16 v[126:127], %[ub] offset:3872\n" \
  "v_add_f32_dpp v209, v210, v209 wave_shr:1 row_mask:0xf bank_mask:0xf bound_ctrl:1\n" \
  "v_cvt_pk_bf16_f32 v232, v224, v225\n" \
  "v_cvt_pk_bf16_f32 v233, v226, v227\n" \
  "v_mfma_f32_16x16x32_bf16 v[216:219], v[140:143], v[108:111], v[216:219]\n" \
  "v_add_f32_dpp v208, v209, v208 wave_shr:1 row_mask:0xf bank_mask:0xf bound_ctrl:1\n" \
  "global_store_dwordx2 %[yo], v[232:233], %[o2]\n" \
  "v_sub_u32 %[crd], %[crd], %[cst]\n" \
  "v_mfma_f32_16x16x32_bf16 v[212:215], v[168:171], v[76:79], v[212:215]\n" \
  "v_mov_b32_dpp v209, v210 row_shl:4 row_mask:0x8 bank_mask:0x4\n" \
  "v_mov_b32_dpp v209, v211 row_shl:8 row_mask:0x8 bank_mask:0x2\n" \
  "s_setprio 2\n" \
  "v_mfma_f32_16x16x32_bf16 v[216:219], v[144:147], v[104:107], v[216:219]\n" \
  "ds_write_addtid_b32 v208 offset:6144\n" \
  "s_mov_b64 exec, %[l63]\n" \
  "ds_write_b32 %[cw], v209 offset:384\n" \
  "s_mov_b64 exec, -1\n" \
  "v_mfma_f32_16x16x32_bf16 v[212:215], v[172:175], v[72:75], v[212:215]\n" \
  "v_mfma_f32_16x16x32_bf16 v[216:219], v[148:151], v[100:103], v[216:219]\n" \
  "v_mfma_f32_16x16x32_bf16 v[212:215], v[176:179], v[68:71], v[212:215]\n" \
  "v_mfma_f32_16x16x32_bf16 v[216:219], v[152:155], v[96:99], v[216:219]\n" \
  "s_waitcnt lgkmcnt(4)\n" \
  "v_mfma_f32_16x16x32_bf16 v[212:215], v[180:183], v[64:67], v[212:215]\n" \
  "v_mfma_f32_16x16x32_bf16 v[216:219], v[156:159], v[92:95], v[216:219]\n" \
  "s_waitcnt lgkmcnt(2)\n" \
  "v_mfma_f32_16x16x32_bf16 v[220:223], v[128:131], v[124:127], 0\n" \
  "v_mfma_f32_16x16x32_bf16 v[216:219], v[160:163], v[88:91], v[216:219]\n" \
  "s_setprio 1\n" \
  "v_mfma_f32_16x16x32_bf16 v[220:223], v[132:135], v[120:123], v[220:223]\n" \
  "v_mfma_f32_16x16x32_bf16 v[216:219], v[164:167], v[84:87], v[216:219]\n" \
  "ds_read_b64_tr_b16 v[184:185], %[kb] offset:3616\n" \
  "ds_read_b64_tr_b16 v[186:187], %[kb] offset:3584\n" \
  "v_mfma_f32_16x16x32_bf16 v[220:223], v[136:139], v[116:119], v[220:223]\n" \
  "v_add_f32_dpp v214, v215, v214 wave_shr:1 row_mask:0xf bank_mask:0xf bound_ctrl:1\n" \
  "v_mfma_f32_16x16x32_bf16 v[216:219], v[168:171], v[80:83], v[216:219]\n" \
  "s_nop 0\n" \
  "v_add_f32_dpp v213, v214, v213 wave_shr:1 row_mask:0xf bank_mask:0xf bound_ctrl:1\n" \
  "v_mfma_f32_16x16x32_bf16 v[220:223], v[140:143], v[112:115], v[220:223]\n" \
  "s_nop 0\n" \
  "v_add_f32_dpp v212, v213, v212 wave_shr:1 row_mask:0xf bank_mask:0xf bound_ctrl:1\n" \
  "v_mfma_f32_16x16x32_bf16 v[216:219], v[172:175], v[76:79], v[216:219]\n" \
  "v_mov_b32_dpp v213, v214 row_shl:4 row_mask:0x8 bank_mask:0x4\n" \
  "v_mov_b32_dpp v213, v215 row_shl:8 row_mask:0x8 bank_mask:0x2\n" \
  "s_setprio 0\n" \
  "v_mfma_f32_16x16x32_bf16 v[220:223], v[144:147], v[108:111], v[220:223]\n" \
  "ds_write_addtid_b32 v212 offset:6656\n" \
  "s_mov_b64 exec, %[l63]\n" \
  "ds_write_b32 %[cw], v213 offset:256\n" \
  "s_mov_b64 exec, -1\n" \
  "v_mfma_f32_16x16x32_bf16 v[216:219], v[176:179], v[72:75], v[216:219]\n" \
  "v_mfma_f32_16x16x32_bf16 v[220:223], v[148:151], v[104:107], v[220:223]\n" \
  "s_waitcnt lgkmcnt(0)\n" \
  "s_barrier\n" \
  "s_setprio 3\n" \
  "ds_read2st64_b32 v[224:225], %[prd] offset0:24 offset1:56\n" \
  "ds_read2st64_b32 v[226:227], %[prd] offset0:88 offset1:120\n" \
  "ds_read_b128 v[228:231], %[crd]\n" \
  "v_mfma_f32_16x16x32_bf16 v[216:219], v[180:183], v[68:71], v[216:219]\n" \
  "v_mfma_f32_16x16x32_bf16 v[220:223], v[152:155], v[100:103], v[220:223]\n" \
  "ds_read_b64_tr_b16 v[188:189], %[kb] offset:3872\n" \
  "ds_read_b64_tr_b16 v[190:191], %[kb] offset:3840\n" \
  "v_mfma_f32_16x16x32_bf16 v[216:219], v[184:187], v[64:67], v[216:219]\n" \
  "v_mfma_f32_16x16x32_bf16 v[220:223], v[156:159], v[96:99], v[220:223]\n" \
  "v_mfma_f32_16x16x32_bf16 v[220:223], v[160:163], v[92:95], v[220:223]\n" \
  "s_setprio 2\n" \
  "v_mfma_f32_16x16x32_bf16 v[220:223], v[164:167], v[88:91], v[220:223]\n" \
  "s_waitcnt lgkmcnt(2)\n" \
  "v_add_f32 v224, v224, v228\n" \
  "v_add_f32 v225, v225, v229\n" \
  "v_mfma_f32_16x16x32_bf16 v[220:223], v[168:171], v[84:87], v[220:223]\n" \
  "v_add_f32 v226, v226, v230\n" \
  "v_add_f32 v227, v227, v231\n" \
  "v_mfma_f32_16x16x32_bf16 v[220:223], v[172:175], v[80:83], v[220:223]\n" \
  "v_cvt_pk_bf16_f32 v232, v224, v225\n" \
  "v_cvt_pk_bf16_f32 v233, v226, v227\n" \
  "s_setprio 1\n" \
  "v_mfma_f32_16x16x32_bf16 v[220:223], v[176:179], v[76:79], v[220:223]\n" \
  "v_add_f32_dpp v218, v219, v218 wave_shr:1 row_mask:0xf bank_mask:0xf bound_ctrl:1\n" \
  "s_mov_b64 exec, %[m0]\n" \
  "global_store_dwordx2 %[yo], v[232:233], %[o3]\n" \
  "s_mov_b64 exec, -1\n" \
  "v_mfma_f32_16x16x32_bf16 v[220:223], v[180:183], v[72:75], v[220:223]\n" \
  "v_add_f32_dpp v217, v218, v217 wave_shr:1 row_mask:0xf bank_mask:0xf bound_ctrl:1\n" \
  "v_mfma_f32_16x16x32_bf16 v[220:223], v[184:187], v[68:71], v[220:223]\n" \
  "s_nop 0\n" \
  "v_add_f32_dpp v216, v217, v216 wave_shr:1 row_mask:0xf bank_mask:0xf bound_ctrl:1\n" \
  "s_setprio 0\n" \
  "s_waitcnt lgkmcnt(0)\n" \
  "v_mfma_f32_16x16x32_bf16 v[220:223], v[188:191], v[64:67], v[220:223]\n" \
  "v_mov_b32_dpp v217, v218 row_shl:4 row_mask:0x8 bank_mask:0x4\n" \
  "v_mov_b32_dpp v217, v219 row_shl:8 row_mask:0x8 bank_mask:0x2\n" \
  "ds_write_addtid_b32 v216 offset:7168\n" \
  "s_mov_b64 exec, %[l63]\n" \
  "ds_write_b32 %[cw], v217 offset:128\n" \
  "s_mov_b64 exec, -1\n" \
  "s_nop 1\n" \
  "v_add_f32_dpp v222, v223, v222 wave_shr:1 row_mask:0xf bank_mask:0xf bound_ctrl:1\n" \
  "s_nop 1\n" \
  "v_add_f32_dpp v221, v222, v221 wave_shr:1 row_mask:0xf bank_mask:0xf bound_ctrl:1\n" \
  "s_nop 1\n" \
  "v_add_f32_dpp v220, v221, v220 wave_shr:1 row_mask:0xf bank_mask:0xf bound_ctrl:1\n" \
  "v_mov_b32_dpp v221, v222 row_shl:4 row_mask:0x8 bank_mask:0x4\n" \
  "v_mov_b32_dpp v221, v223 row_shl:8 row_mask:0x8 bank_mask:0x2\n" \
  "ds_write_addtid_b32 v220 offset:7680\n" \
  "s_mov_b64 exec, %[l63]\n" \
  "ds_write_b32 %[cw], v221 offset:0\n" \
  "s_mov_b64 exec, -1\n" \
  "s_waitcnt lgkmcnt(0)\n" \
  "s_barrier\n" \
  "ds_read2st64_b32 v[224:225], %[prd] offset0:24 offset1:56\n" \
  "ds_read2st64_b32 v[226:227], %[prd] offset0:88 offset1:120\n" \
  "ds_read_b128 v[228:231], %[crd]\n" \
  "s_waitcnt lgkmcnt(0)\n" \
  "v_add_f32 v224, v224, v228\n" \
  "v_add_f32 v225, v225, v229\n" \
  "v_add_f32 v226, v226, v230\n" \
  "v_add_f32 v227, v227, v231\n" \
  "v_cvt_pk_bf16_f32 v232, v224, v225\n" \
  "v_cvt_pk_bf16_f32 v233, v226, v227\n" \
  "s_mov_b64 exec, %[m1]\n" \
  "global_store_dwordx2 %[yo], v[232:233], %[o3]\n" \
  "s_mov_b64 exec, -1\n"
#define CONV_CLOB "v64", "v65", "v66", "v67", "v68", "v69", "v70", "v71", "v72", "v73", "v74", "v75", "v76", "v77", "v78", "v79", "v80", "v81", "v82", "v83", "v84", "v85", "v86", "v87", "v88", "v89", "v90", "v91", "v92", "v93", "v94", "v95", "v96", "v97", "v98", "v99", "v100", "v101", "v102", "v103", "v104", "v105", "v106", "v107", "v108", "v109", "v110", "v111", "v112", "v113", "v114", "v115", "v116", "v117", "v118", "v119", "v120", "v121", "v122", "v123", "v124", "v125", "v126", "v127", "v128", "v129", "v130", "v131", "v132", "v133", "v134", "v135", "v136", "v137", "v138", "v139", "v140", "v141", "v142", "v143", "v144", "v145", "v146", "v147", "v148", "v149", "v150", "v151", "v152", "v153", "v154", "v155", "v156", "v157", "v158", "v159", "v160", "v161", "v162", "v163", "v164", "v165", "v166", "v167", "v168", "v169", "v170", "v171", "v172", "v173", "v174", "v175", "v176", "v177", "v178", "v179", "v180", "v181", "v182", "v183", "v184", "v185", "v186", "v187", "v188", "v189", "v190", "v191", "v192", "v193", "v194", "v195", "v196", "v197", "v198", "v199", "v200", "v201", "v202", "v203", "v204", "v205", "v206", "v207", "v208", "v209", "v210", "v211", "v212", "v213", "v214", "v215", "v216", "v217", "v218", "v219", "v220", "v221", "v222", "v223", "v224", "v225", "v226", "v227", "v228", "v229", "v230", "v231", "v232", "v233"
struct Op { int op, d, a, b; };
constexpr int P0 = 60;
constexpr Op OPS[] = {
  {2, -1, 8, 0}, {1, 0, 8, 0}, {2, -1, 0, 0}, {1, 0, 0, 0},
  {0, 48, 32, P0 + 8}, {2, -1, 9, 0}, {1, 0, 9, 0}, {0, 33, 32, P0}, {2, -1, 1, 0}, {1, 0, 1, 0},
  {0, 50, 32, P0 + 9}, {0, 54, 48, P0 + 9}, {2, -1, 10, 0}, {2, -1, 2, 0}, {1, 0, 2, 0},
  {0, 34, 32, P0 + 1}, {0, 35, 33, P0 + 1}, {2, 1, 3, 0}, {1, 0, 3, 0},
  {0, 49, 48, P0 + 1}, {0, 43, 32, P0 + 10}, {2, 2, 4, 0}, {1, 0, 4, 0},
  {0, 40, 48, P0 + 10}, {0, 47, 50, P0 + 10}, {2, 4, 5, 0}, {1, 0, 5, 0},
  {0, 44, 54, P0 + 10}, {0, 41, 43, P0 + 10}, {2, 8, 6, 0}, {1, 0, 6, 0},
  {0, 36, 32, P0 + 2}, {0, 37, 33, P0 + 2}, {2, 16, 7, 0},
  {0, 3, 1, 2}, {0, 38, 34, P0 + 2}, {0, 39, 35, P0 + 2}, {0, 52, 48, P0 + 2}, {0, 53, 49, P0 + 2}, {0, 51, 50, P0 + 2}, {0, 55, 54, P0 + 2},
  {0, 5, 1, 4}, {0, 6, 2, 4}, {0, 7, 3, 4},
  {0, 9, 1, 8}, {0, 10, 2, 8}, {0, 11, 3, 8}, {0, 12, 4, 8}, {0, 13, 5, 8}, {0, 14, 6, 8}, {0, 15, 7, 8},
  {0, 17, 1, 16}, {0, 18, 2, 16}, {0, 19, 3, 16}, {0, 20, 4, 16}, {0, 21, 5, 16}, {0, 22, 6, 16}, {0, 23, 7, 16}, {0, 24, 8, 16},
  {0, 25, 9, 16}, {0, 26, 10, 16}, {0, 27, 11, 16}, {0, 28, 12, 16}, {0, 29, 13, 16}, {0, 30, 14, 16}, {0, 31, 15, 16}};
constexpr int NOPS = sizeof(OPS) / sizeof(Op);

#define STAMP(i)
#define QSTAMP(i)
#define PSTAMP(i)
#define STAMP_OUT()
#define CNT()
#define CNT2()

#define RSTAMP(i)
#define OUTM ""
#define ENDW 0
#define TBSTAMP()
#define FINISH() do { if (ENDW) WAITV(0); STAMP(11); STAMP_OUT(); __builtin_amdgcn_endpgm(); } while (0)
typedef u32 u32x8 __attribute__((ext_vector_type(8)));
typedef u32 u32x16 __attribute__((ext_vector_type(16)));
__global__ void __launch_bounds__(512, 2)
fused_kernel(const char* a_u, const char* a_Are, const char* a_Aim, const char* a_Bre, const char* a_Bim, const char* a_Cre,
             const char* a_Cim, const char* a_logdt, const char* a_dskip, const char* a_x, const char* a_wqkv,
             const char* a_wo, char* a_out, char* a_ws) {
  __shared__ __attribute__((aligned(16))) char smem[LDS_BYTES];
  LAS char* sm = (LAS char*)smem;
  u32x16 ka0;
  u32x8 ka1;
  u32x4 ka2;
  asm volatile("s_load_dwordx8 %1, %3, 0x40\n\ts_load_dwordx16 %0, %3, 0x0\n\ts_load_dwordx4 %2, %3, 0x60"
               : "=&s"(ka0), "=&s"(ka1), "=&s"(ka2) : "s"(__builtin_amdgcn_kernarg_segment_ptr()));
  const int tid = threadIdx.x, lane = tid & 63;
  const int wave = __builtin_amdgcn_readfirstlane(tid >> 6);
  const int xc = blockIdx.x & 7, idx = blockIdx.x >> 3;
  STAMP(0);
  const u32 sm0 = (u32)(uintptr_t)sm;
  u64 pc;
  asm volatile("s_getpc_b64 %0" : "=s"(pc));
  if (wave == 3 && idx < 24 && (idx & 7) * 8192 < CODE_BYTES) touch((gptr)pc + min((u32)((idx & 7) * 8192 + lane * 128), (u32)CODE_BYTES), sm0 + JUNK);
  const int g = lane >> 4, q = (lane >> 2) & 3, p = lane & 3, l15 = lane & 15;
  const int lw = wave & 3, jj = idx & 15;
  const int rb = (xc >> 1) * 4 + (idx & 3), hp = (xc & 1) * 4 + ((idx >> 2) & 3);
  const int an = (xc - (xc > XS)) * 16 + jj;
  const int gam = an >> 4, mu = an & 15;
  u32 pre[3];
  const bool loader = idx < 16 && wave >= 4;
  const u32 bc = idx < 16 ? 0u : ((xc != XS ? TORD : SORD) >> (4 * wave)) & 15u;
  if (loader) {
    pre[2] = (u32)(rb * 128 + lw * 32 + (lane >> 3)) * 896u + (u32)(((lane & 7) ^ (lane >> 4)) << 4);
#pragma unroll
    for (int i = 0; i < 2; i++) {
      u32 e = (u32)(i * 256 + (tid & 255));
      u32 rho = e / 14u, r = e - rho * 14u;
      u32 k = (rho & ~12u) | ((rho & 4u) << 1) | ((rho >> 1) & 4u);
      pre[i] = k * 2688u + (r >= 7u ? 1792u - 112u : 896u) + r * 16u + (u32)hp * 112u;
    }
  } else {
    pre[0] = bline(bc, lane, jj, xc);
    pre[1] = (u32)((an * 4 + lw) * 512 + (wave >> 2) * 256 + lane * 4);
    pre[2] = (u32)(an * 8 + (lw & 2) * 2);
  }
  asm volatile("" : "+v"(pre[0]), "+v"(pre[1]), "+v"(pre[2]));
  asm volatile("s_waitcnt lgkmcnt(0)" : "+s"(ka0), "+s"(ka1), "+s"(ka2));
#define ARG(v, i) ((gptr)(((u64)v[2 * (i) + 1] << 32) | v[2 * (i)]))
  const gptr P_u = ARG(ka0, 0), P_logdt = ARG(ka0, 7), P_dskip = ARG(ka1, 0), P_x = ARG(ka1, 1), P_wqkv = ARG(ka1, 2), P_wo = ARG(ka1, 3), P_out = ARG(ka2, 0);
  PSTAMP(6);
  const gptr ws = ARG(ka2, 1);
  const auto wsr = __builtin_amdgcn_make_buffer_rsrc((void*)ws, 0, WS_BYTES, 0x00020000);
  LAS u32* const m_cnt = (LAS u32*)(sm + MISC);
  const gptr rep = ws + WS_REP + xc * 4096;
  const u32 px1 = pre[2] ^ 64u;
  const u32 rot = (ROT >> (4 * ((idx + (idx >> 2)) & 3))) & 7u;
  auto chunk = [&](int c) { const u32 k = rot + c; return k >= 7u ? k - 7u : k; };
  auto ldx = [&](int c) {
    const gptr b = P_x + chunk(c) * 128u;
#pragma unroll
    for (int i = 0; i < 4; i++) dma16(b + i * (8 * 896), (i & 1) ? px1 : pre[2], sm0 + slot_of(c) + lw * 4096 + i * 1024);
  };
  auto ldw = [&](int c) {
    const u32 d = sm0 + slot_of(c) + 16384 + lw * 1024;
    const gptr b = P_wqkv + chunk(c) * (64u * 2688u);
#pragma unroll
    for (int i = 0; i < 2; i++) dma16(b + i * (32 * 2688), pre[0], d + i * 7168);
    if (lw < 3) {
#pragma unroll
      for (int i = 0; i < 2; i++) dma16(b + i * (32 * 2688), pre[1], d + 4096 + i * 7168);
    }
  };
  const gptr ba = (lane < 32 && bc < 7 ? P_x : P_wqkv) + pre[0];
  const u32 pl = A_U + 32 + lw * U_PITCH + (wave >> 2) * 2048;
  if (loader) { ldx(0); ldw(0); }
  else {
    if ((((idx < 16 ? 1 : xc != XS ? M0 : MS0) >> wave) & 1)) touch(ba, sm0 + JUNK);
    if (idx >= 16 && xc != XS) {
#pragma unroll
      for (int i = 0; i < 6; i++) dma4n(ARG(ka0, 1 + i), pre[1], sm0 + pl + i * 256);
      dma4n(P_logdt, pre[2], sm0 + pl + 6 * 256);
    }
  }
  if ((EXP & 1) && idx >= 16) { WAITV(0); FINISH(); }
  u64 T;
  auto epoch_ld = [&]() { asm volatile("s_load_dwordx2 %0, %1, 0x0" : "=s"(T) : "s"(ws + WS_EPOCH)); };
  auto epoch_get = [&]() { asm volatile("s_waitcnt lgkmcnt(0)" : "+s"(T)); T += EPOCH_INC; };
  auto tag = [&]() { return ((u32)T << 16) & 0xbfff0000u; };
  auto last = [&]() {
    u32 n = 0;
    if (lane == 0) n = __hip_atomic_fetch_add(m_cnt, 1u, RLX_WG);
    return rfl(n) == 7;
  };

  if (__builtin_expect(idx < 16, EXB)) {
    const u32 fa_ = sm0 + L_FULL + p * 4;
    const int rt = xc * 4 + (idx & 3), hf = wave >> 2, hs = hf ^ SWP;
    u32 wa[5], oo, lo, kr, so_pq[4];
    auto mk_wa = [&]() {
      wa[1] = (4 * g + q) * 224 + (8 + hf) * 6272 + 8 * p;
      wa[3] = ((q >> 1) * 56 + 16 + 2 * (g ^ (q & 2)) + (q & 1)) * 224 + (8 + hf) * 6272 + 8 * p;
      asm volatile("" : "+v"(wa[1]), "+v"(wa[3]));
    };
    auto mk_b3 = [&]() {
      wa[0] = wa[1] - 8 * 6272;
      wa[2] = wa[3] - 8 * 6272;
      wa[4] = (q * 56 + 24 + (g ^ (q & 1) * 2)) * 224 + hf * 6272 + 8 * p;
      oo = (u32)((rt * 64 + lw * 16 + (lane >> 3)) * 1792 + 896 + (idx >> 2) * 224 + hs * 128 + (lane & 7) * 16);
      lo = lane * 16;
      kr = L_O + hf * 2048 + lane * 16;
      asm volatile("" : "+v"(oo), "+v"(lo), "+v"(kr), "+v"(wa[0]), "+v"(wa[1]), "+v"(wa[2]), "+v"(wa[3]), "+v"(wa[4]));
    };
    u32x4 wo[49];
    constexpr auto mk = [](int m) { return m < 28 ? (0x1523 >> (m / 7 * 4)) & 7 : (0x064 >> ((m - 28) / 7 * 4)) & 7; };
    constexpr auto mc = [](int m) { return m % 7; };
    auto ldwo = [&](int m) __attribute__((always_inline)) {
      const int j = mk(m);
      LAS char* a = sm + wa[j < 4 ? j >> 1 : j - 2] + (j < 4 ? (j & 1) * 25088 : 0) + mc(m) * 32;
      wo[m] = trd2(a, a + (j < 4 ? 12544 : j < 6 ? 25088 : 50176));
      SCHED();
    };
    const u32 fb_ = sm0 + L_FULL + (lane & 7) * 4;
    u32 th[3];
    auto hints = [&]() __attribute__((always_inline)) {
      const u32 v = idle_wait<14 * 16>(fa_);
#pragma unroll
      for (int i = 0; i < 3; i++) th[i] = __builtin_amdgcn_readlane(v, i < 2 ? i : 2 + hf);
    };
    u32 owo[7];
    auto mk_wo = [&](int i, u32 e) { owo[i] = (e / 14u) * (896u - 224u) + e * 16u + (u32)(idx >> 2) * 224u; pin(owo[i]); };
    if (EXL < 0 ? wave >= 4 : __builtin_expect(wave >= 4, EXL)) {
      __builtin_amdgcn_s_setprio(LPR);
      PSTAMP(7);
      BAR();
      epoch_ld();
      PSTAMP(8);
      u32x4 xq[28];
      u32 rt0, cy0;
      const int l31 = lane & 31, hh = lane >> 5;
      u32 xa0 = (lw * 32 + l31) * 128 + ((hh ^ (l15 >> 1)) << 4), oq;
      auto capture = [&](int c) {
#pragma unroll
        for (int ks = 0; ks < 4; ks++) xq[4 * c + ks] = *(LAS u32x4*)(sm + slot_of(c) + (xa0 ^ (ks * 32)));
      };
      u32 wq, sq, wsel;
      gptr pqp, wop;
      f32x16 aq[2];
      auto prep = [&]() __attribute__((always_inline)) {
        wq = L_WQ + (4 * q + 2 * (g >> 1)) * 112 + (g & 1) * 32 + 8 * p; sq = (u32)((lane & 16) * 1024 + hh * 256 + l15 * 16);
        wsel = hh ? 0x0c0c0302u : 0x0c0c0504u;
        pqp = ws + WS_PQ + ((rb * 8 + lw * 2) * 16 + hp * 2) * 1024; wop = P_wo + 12 * 28672;
        asm volatile("" : "+v"(wq), "+v"(sq), "+v"(wsel));
        asm volatile("" : "+s"(pqp), "+s"(wop));
        mk_wo(0, tid & 255);
        mk_wo(1, 256 + (tid & 255));
        mk_wa();
#pragma unroll
        for (int i = 0; i < 16; i++) aq[0][i] = 1.000001f;
        pin(aq[0]);
        aq[1] = aq[0];
        if (PB3) mk_b3();
      };
      auto ldwq = [&](int n) { dma16m(P_wqkv + chunk(n >> 1) * (64u * 2688u) + (n & 1) * (32 * 2688), oq, sm0 + L_WQ + n * 3584 + lw * 896); };
#pragma unroll
      for (int c = 0; c < 7; c++) {
        if (c) ldx(c);
        if (c >= 2) { WAITV(PACE); if (c == 4) epoch_get(); }
        if (c == RC0) { rt0 = now(); cy0 = (u32)__builtin_amdgcn_s_memtime(); }
        if (c >= 2) capture(c - 2);
        if (c) ldw(c);
        if (c == 5) asm volatile("s_waitcnt lgkmcnt(0)" ::: "memory");
        dma_mark(pc, sm0 + L_FULL + c * 16 + lw * 4);
        if (EXP & 512) dma_mark(pc, sm0 + JUNK);
        if (c == 0) STAMP(1);
        if (c == 1) {
          const u32 i = (u32)lane / 7u;
          oq = ((lw >> 1) * 16 + (lw & 1) * 2 + (i & 3u) * 4u + (i >> 2)) * 2688u + (lane - i * 7u) * 16u + (u32)hp * 112u;
          pin(oq);
        }
        if (c == HUP && lw == 3) {
          const u32 rt1 = now(), cy1 = (u32)__builtin_amdgcn_s_memtime();
          const float rho = (float)(rt1 - rt0) * __builtin_amdgcn_rcpf((float)(cy1 - cy0));
          const float ex = KPR * (float)(rt1 - rt0);
          const u32 e = ((rt1 + (u32)__builtin_fmaf(C_RED, rho, ex)) & 0xffffu) | (rt1 + (u32)__builtin_fmaf(C_PQ1, rho, ex)) << 16;
          if (lane < 8) st8(ws + WS_REP + lane * 4096 + (hp * 16 + rb) * 8, ((u64)e << 32) | (u32)T);
        }
        if (c == PREP) prep();
        if (c == 4) BAR();
      }
      STAMP(2);
#pragma unroll
      for (int n = 0; n < 8; n++) {
        ldwq(n);
        if (n == 3) { WAITV(PACE); capture(5); }
      }
      asm volatile("s_waitcnt lgkmcnt(0)" ::: "memory");
      dma_mark(pc, sm0 + L_FULL + 7 * 16 + lw * 4);
      arrive_wait<9 * 16>(fa_, 0u);
#pragma unroll
      for (int n = 8; n < 8 + NQB; n++) ldwq(n);
      if (!M8) dma_mark(pc, sm0 + L_FULL + 8 * 16 + lw * 4);
      if (PREP > 6) prep();
      const u32 tg = tag();
      arrive_wait<6 * 16>(fa_, 0u);
      PSTAMP(9);
      capture(6);
      *(LAS u32*)(sm + L_FULL + 15 * 16 + lw * 4) = 1u;
      arrive_wait<7 * 16>(fa_, 0u);
      PSTAMP(10);
      arrive_wait<10 * 16>(fa_, 0u);
#pragma unroll
      for (int n = 0; n < 2; n++) dma16(wop + n * 28672, owo[0], sm0 + (12 + n) * 7168 + lw * 1024);
      if (lw < 3) {
#pragma unroll
        for (int n = 0; n < 2; n++) dma16(wop + n * 28672, owo[1], sm0 + (12 + n) * 7168 + 4096 + lw * 1024);
      }
      dma_mark(pc, sm0 + L_FULL + 11 * 16 + lw * 4);
      u32x4 wf[6];
      constexpr auto qh = [](int s) { return s < 16 ? 0 : s < 16 + QX ? 1 : s < 28 + QX ? 0 : 1; };
      constexpr auto qk = [](int s) { return s < 16 ? s : s < 16 + QX ? s - 16 : s < 28 + QX ? s - QX : s - 28; };
      auto ldq = [&](int s) {
        LAS char* a = sm + wq + qk(s) * 1792 + qh(s) * 56;
        wf[s % 6] = trd2(a, a + 112);
      };
#pragma unroll
      for (int s = 0; s < 5; s++) ldq(s);
      u32 fl;
      u32 ph[4];
      float pe[2];
      auto php = [&](int hd, int k, int part) {
        const int r = (k & 4) + (k & 2) * 4 + (k & 1) * 2;
        if (part < 2) { pe[part] = phie<true>(aq[hd][r + part]); pin(pe[part]); }
        else ph[k & 3] = phim(aq[hd][r], aq[hd][r + 1], pe[0] + 1e-6f, pe[1] + 1e-6f);
      };
      auto phq = [&](int hd, int k) {
        const int r = (k & 4) + (k & 2) * 4 + (k & 1) * 2;
        ph[k & 3] = phi2<true>(aq[hd][r], aq[hd][r + 1]);
      };
      u32 w0;
      auto stq = [&](int hd, int b) {
        if (b == 0) { w0 = ph[3]; ph[3] &= 0xffffu; }
        else {
          const u32 x = __builtin_amdgcn_perm(ph[3], w0, 0x07060302u);
          const auto y = __builtin_amdgcn_permlane32_swap(x, x, false, false);
          ph[2] = hh ? y[0] : ph[2];
          ph[3] = __builtin_amdgcn_perm(ph[3], w0, wsel);
        }
        st16(pqp + hd * 1024 + b * 512, sq, u32x4{ph[0], ph[1], ph[2], ph[3] | tg});
      };
      idle_wait<16 * 16>(fa_);
      STAMP(3);
      SCHED();
      constexpr auto npw = [](int s) { const int n = s < QW ? 0 : s - QW + 1 + (s < QW2 ? 0 : s - QW2 + 1); return n < 21 ? n : 21; };
      static_assert(npw(55) == 21, "");
      static_for<0, 56>([&](auto S) {
        constexpr int s = S.v;
        aq[qh(s)] = mfma32(wf[s % 6], xq[qk(s)], aq[qh(s)]);
        SCHED();
        if constexpr (s == 6 + QX) fl = *(LAS u32*)(sm + L_FULL + p * 4 + (M8 ? 11 : 8) * 16);
        if constexpr (s == 11 + QX) arrive_wait<(M8 ? 11 : 8) * 16>(fa_, fl);
        if constexpr (s + 5 < 56) ldq(s + 5);
        if constexpr (s == QW - 4) fl = *(LAS u32*)(uintptr_t)(fb_ + 11 * 16);
        if constexpr (s == QW) arrive_wait<11 * 16>(fb_, fl);
        if constexpr (s >= QW && npw(s) > npw(s - 1)) ldwo(npw(s - 1));
        if constexpr (s >= QW && npw(s) > npw(s - 1) + 1) ldwo(npw(s - 1) + 1);
        if constexpr (s == KVS) *(LAS u32*)(sm + L_FULL + 13 * 16 + lw * 4) = 1u;
        if constexpr (s == 50) *(LAS u32*)(sm + L_FULL + 19 * 16 + lw * 4) = 1u;
        static_assert(QX == 0, "");
        if constexpr (PHP == 3) {
          if constexpr (s >= 30 && s < 42) php(0, (s - 30) / 3, (s - 30) % 3);
          if constexpr (s == 42) stq(0, 0);
          if constexpr (s >= 43 && s < 55) php(0, 4 + (s - 43) / 3, (s - 43) % 3);
          if constexpr (s == 55) stq(0, 1);
        } else {
          if constexpr (s >= 30 && s < 38) { php(0, (s - 30) / 2, s & 1); if constexpr (s & 1) php(0, (s - 30) / 2, 2); }
          if constexpr (s == 38) stq(0, 0);
          if constexpr (s >= 39 && s < 47) { php(0, 4 + (s - 39) / 2, !(s & 1)); if constexpr (!(s & 1)) php(0, 4 + (s - 39) / 2, 2); }
          if constexpr (s == 47) stq(0, 1);
        }
        if constexpr (s == 8) QSTAMP(6);
        if constexpr (s == 18) QSTAMP(7);
        if constexpr (s == 30) QSTAMP(8);
        if constexpr (s == 42) QSTAMP(9);
        if constexpr (s == 50) QSTAMP(10);
        SCHED();
      });
      STAMP(4);
#pragma unroll
      for (int k = 0; k < 4; k++) phq(1, k);
      stq(1, 0);
#pragma unroll
      for (int k = 4; k < 8; k++) phq(1, k);
      stq(1, 1);
      __builtin_amdgcn_s_setprio(0);
      STAMP(5);
      SCHED();
      if (!PB3) mk_b3();
      hints();
    } else {
      const int t2 = wave & 1, hd = wave >> 1;
      gptr pb = ws + WS_PART + ((hp * 2 + hd) * 256 + rb * 2 + t2) * 256, pb1 = pb + 32768;
      if (wave == 1) *(LAS u32x2*)(sm + L_FULL + lane * 8) = u32x2{0u, 0u};
      PSTAMP(7);
      BAR();
      epoch_ld();
      SCHED();
      PSTAMP(8);
      f32x4 acc[2][2][4];
#pragma unroll
      for (int t = 0; t < 2; t++)
#pragma unroll
        for (int tt = 0; tt < 4; tt++) { acc[0][t][tt] = f32x4{1.f, 1.f, 1.f, 1.f}; acc[1][t][tt] = f32x4{0.f, 0.f, 0.f, 0.f}; }
      const int xa0 = (t2 * 64 + l15) * 128 + ((g ^ (l15 >> 1)) << 4);
      const int wa0 = 16384 + (16 * (g >> 1) + 4 * (g & 1) + q) * 224 + 8 * p + hd * 56;
      u32 ra[3][3];
#pragma unroll
      for (int i = 0; i < 3; i++) {
        ra[i][0] = RING + 2 * i * SLOT + xa0; ra[i][1] = RING + 2 * i * SLOT + (xa0 ^ 64); ra[i][2] = RING + 2 * i * SLOT + wa0;
        asm volatile("" : "+v"(ra[i][0]), "+v"(ra[i][1]), "+v"(ra[i][2]));
      }
      u32 so[2];
      so[0] = g * 8192 + l15 * 16;
      so[1] = g * 8192 + ((l15 < 8 ? (l15 >> 1) * 4 + (l15 & 1) : p * 4 + q) ^ (hp & 1) * 8) * 16;
      u32 ysel[2] = {g == 3 ? 0x0c0c0100u : 0x0c0c0504u, g == 3 ? 0x0c0c0302u : 0x0c0c0504u};
      u32 tw = L_FULL + (lane < 4 ? 14 * 16 + lane * 4 : lane < 8 ? 12 * 16 : 18 * 16);
      u32 ha = p == 0 ? A_RED : p == 1 ? A_KV : p == 2 ? A_PQ0 : A_PQ1;
      pin(ha);
      asm volatile("" : "+v"(so[0]), "+v"(so[1]), "+v"(ysel[0]), "+v"(ysel[1]), "+v"(tw));
      asm volatile("" : "+s"(pb), "+s"(pb1));
      mk_wa();
      mk_b3();
#pragma unroll
      for (int i = 0; i < 7; i++) mk_wo(i, i * 64 + lane);
      u32x4 sel;
      {
        const u32 one = (l15 & 1) ? 0x3f800000u : 0x3f80u;
        const int hl = l15 >> 1;
        sel = u32x4{hl == 0 ? one : 0u, hl == 1 ? one : 0u, hl == 2 ? one : 0u, hl == 3 ? one : 0u};
        pin(sel);
      }
      u32x4 eps = {0x35863586u, 0x35863586u, 0x35863586u, 0x35863586u};
      u32 vm = l15 >= 12 ? 0u : ~0u, vo = l15 >= 12 ? 0x3f803f80u : 0u, zf = L_FULL + 16 * 16 + wave * 4;
      asm volatile("" : "+v"(eps), "+v"(vm), "+v"(vo), "+v"(zf));
      u32x4 xf[2][4];
      u32x2 wf[2][8];
      auto rd = [&](int s, int i) {
        const int sl = slot_ix(s >> 1), w = i < 2 ? i : i - 4;
        if ((EXP & 2) && s > 1) return;
        if (i >= 2 && i < 6) xf[s & 1][i - 2] = *(LAS u32x4*)(sm + ra[sl >> 1][s & 1] + (sl & 1) * SLOT + (i - 2) * 2048);
        else wf[s & 1][w] = trd(sm + ra[sl >> 1][2] + (sl & 1) * SLOT + (s & 1) * (32 * 224) + (w >> 2) * 112 + ((w >> 1) & 1) * 32 + (w & 1) * (8 * 224));
      };
      auto mm = [&](int s, int n) {
        const int i = n >> 2, tt = n & 3;
        acc[i >> 1][i & 1][tt] = mfma16(xf[s & 1][tt], cat(wf[s & 1][2 * i], wf[s & 1][2 * i + 1]), acc[i >> 1][i & 1][tt]);
      };
      PSTAMP(9);
      epoch_get();
      arrive_wait<0>(fa_, 0u);
      __builtin_amdgcn_s_setprio(3);
      STAMP(1);
#pragma unroll
      for (int i = 0; i < 12; i++) rd(0, i);
      SCHED();
      static_for<0, 12>([&](auto S) {
        constexpr int s = S.v, c = (s + 1) >> 1, wp = !(s & 1) ? 0 : s == 11 ? WPL : WP, rr = wp > 4 ? 2 : 1;
        u32 fl;
#pragma unroll
        for (int n = 0; n < 16; n++) {
          if ((s & 1) && n == (wp > 3 ? wp - 3 : 0)) fl = *(LAS u32*)(sm + L_FULL + p * 4 + c * 16);
          mm(s, n);
          if ((s & 1) && n == wp) {
            SCHED();
            if constexpr (c == 3) BAR();
            arrive_wait<c * 16>(fa_, fl);
            if constexpr (c >= 5) *(LAS u32*)(sm + L_FULL + (c + 4) * 16 + wave * 4) = 1u;
            if constexpr (c <= 5) QSTAMP(5 + c);
          }
#pragma unroll
          for (int k = 0; k < rr; k++)
            if (n >= wp && (n - wp) * rr + k < 12) rd(s + 1, (n - wp) * rr + k);
          SCHED();
        }
      });
      {
        u32 pkf[2][2][4], vf[2][2][4];
        f32x4 d[2][2];
        auto vq = [&](int t, int k2) { return u32x4{vf[t][k2][0], vf[t][k2][1], vf[t][k2][2], vf[t][k2][3]}; };
        auto kq = [&](int t, int k2) { return u32x4{pkf[t][k2][0], pkf[t][k2][1], pkf[t][k2][2], pkf[t][k2][3]}; };
#pragma unroll
        for (int n = 0; n < 12; n++) { mm(12 + (n >> 3), n & 7); rd(13, n); SCHED(); }
        STAMP(2);
        static_for<0, 16>([&](auto K) {
          constexpr int k = K.v, td = k >> 3, tt = (k >> 1) & 3;
          auto mf = [&](int i) {
            if constexpr (k < 2) mm(13, 4 + 2 * k + i);
            else if constexpr (k < 10) mm(12 + ((k - 2) >> 1 & 1), 8 + (k - 2 & 4) + (k & 1) * 2 + i);
            else if constexpr (k == 10 || k == 13) d[1][k & 1] = mfma16(eps, vq(k & 1, i), i ? d[1][k & 1] : f32x4{0.f, 0.f, 0.f, 0.f});
            else if constexpr (k == 11 || k == 14) d[0][k == 14] = mfma16(kq(0, i), vq(k == 14, i), d[!i][k == 14]);
            else if (i == 0) d[1][k & 1] = mfma16(kq(1, 0), vq(k & 1, 0), d[1][k & 1]);
            SCHED();
          };
          if constexpr (k == ZPOS) *(LAS u32*)(sm + zf) = zf;
          if constexpr (k == HRPOS) asm volatile("s_cmp_lg_u32 %3, 0\n\ts_cbranch_scc1 1f\n\ts_mov_b32 m0, %2\n\ts_nop 0\n\tglobal_load_lds_dwordx4 %0, %1 sc1\n1:" ::"v"(lo), "s"(rep), "s"(sm0 + L_TOP + 2048), "s"(wave) : "memory", "scc");
          const float a0 = acc[0][td][tt][2 * (k & 1)], a1 = acc[0][td][tt][2 * (k & 1) + 1];
          mf(0);
          float e0 = phie<false>(a0), e1 = phie<false>(a1);
          SCHED();
          mf(1);
          pkf[td][tt >> 1][k & 3] = phim(a0, a1, e0, e1);
          if constexpr (k == 7 || k == 8 || k == 11 || k == 12) {
            constexpr int t = k > 8, k2 = !(k & 1);
#pragma unroll
            for (int i = 0; i < 4; i++) {
              vf[t][k2][i] = pk(acc[1][t][2 * k2 + (i >> 1)][2 * (i & 1)], acc[1][t][2 * k2 + (i >> 1)][2 * (i & 1) + 1]);
              if (t == 1) vf[t][k2][i] = (vf[t][k2][i] & vm) | vo;
            }
          }
          SCHED();
        });
        d[1][0] = mfma16(kq(1, 1), vq(0, 1), d[1][0]);
        d[1][1] = mfma16(kq(1, 1), vq(1, 1), d[1][1]);
        const u32x2 e0 = pk4(d[0][0]), e1 = pk4(d[0][1]);
        SCHED();
        const u32x2 f0 = pk4(d[1][0]), f1 = pk4(d[1][1]);
        const u32 x = __builtin_amdgcn_perm(f1.y, f0.y, 0x07060302u);
        const u32 x1 = __builtin_amdgcn_permlane32_swap(x, x, false, false)[0];
        const u32 x2 = __builtin_amdgcn_permlane16_swap(x, x, false, false)[0];
        const u32 x0 = __builtin_amdgcn_permlane16_swap(x1, x1, false, false)[0];
        const u32 tg = tag();
        st16(pb, so[0], u32x4{e0.x, e0.y, g == 3 ? __builtin_amdgcn_perm(x2, x0, 0x05040100u) : f0.x, __builtin_amdgcn_perm(f0.y, x1, ysel[0]) | tg});
        st16(pb1, so[1], u32x4{e1.x, e1.y, g == 3 ? __builtin_amdgcn_perm(x2, x0, 0x07060302u) : f1.x, __builtin_amdgcn_perm(f1.y, x1, ysel[1]) | tg});
      }
      STAMP(3);
      if (wave == 0) {
        const u32 t0 = now() - 0x4000u;
        WAITV(2);
        const u32x4 v = poll2(rep + lo, *(LAS u32x4*)(sm + L_TOP + 2048 + lo), (u32)T);
        typedef u16 u16x2 __attribute__((ext_vector_type(2)));
        const u32 vy = v.y, vw = v.w;
        const u16x2 b2 = {(u16)t0, (u16)t0};
        u16x2 m = __builtin_elementwise_max(__builtin_bit_cast(u16x2, vy) - b2, __builtin_bit_cast(u16x2, vw) - b2);
        static_for<0, 4>([&](auto I) { m = __builtin_elementwise_max(m, __builtin_bit_cast(u16x2, __builtin_amdgcn_update_dpp(0, __builtin_bit_cast(int, m), 0x120 + (1 << I.v), 0xf, 0xf, true))); });
        u32 mm_ = __builtin_bit_cast(u32, m);
        { const auto r = __builtin_amdgcn_permlane16_swap(mm_, mm_, false, false); const u32 r0 = r[0], r1 = r[1]; mm_ = __builtin_bit_cast(u32, __builtin_elementwise_max(__builtin_bit_cast(u16x2, r0), __builtin_bit_cast(u16x2, r1))); }
        { const auto r = __builtin_amdgcn_permlane32_swap(mm_, mm_, false, false); const u32 r0 = r[0], r1 = r[1]; mm_ = __builtin_bit_cast(u32, __builtin_elementwise_max(__builtin_bit_cast(u16x2, r0), __builtin_bit_cast(u16x2, r1))); }
        const u32 dh = (t0 + ha + (p < 2 ? mm_ & 0xffffu : mm_ >> 16)) | 1u;
        *(LAS u32*)(sm + tw) = dh;
#pragma unroll
        for (int i = 0; i < 3; i++) th[i] = __builtin_amdgcn_readlane(dh, i);
        TBSTAMP();
        until(th[0]);
        STAMP(5);
        f32x4 s;
#pragma unroll 1
        for (u32 spin = 0; spin < SPIN_CAP; spin++) {
          asm volatile("" ::: "memory");
          u32x4 gr[8];
#pragma unroll
          for (int i = 0; i < 8; i++) gr[i] = __builtin_amdgcn_raw_buffer_load_b128(wsr, lo + (i & 3) * 1024, WS_PART + (hp * 16 + rb) * 8192 + (i >> 2) * 4096, 16);
          SCHED();
          s = f32x4{0.f, 0.f, 0.f, 0.f};
          u32 bad = 0;
#pragma unroll
          for (int i = 0; i < 8; i++) { gr[i].w ^= tag(); bad |= gr[i].w; s = mfma16(sel, gr[i], s); }
          CNT2();
          if (NOCLR || !__any(bad > 0xffffu)) break;
        }
        const u32x2 r = pk4(s);
        const u32 z = __builtin_amdgcn_permlane16_swap(r.x, r.x, false, false)[1], w = __builtin_amdgcn_permlane16_swap(r.y, r.y, false, false)[1];
        asm volatile("s_mov_b64 exec, 0xffff\n\tglobal_store_dwordx4 %0, %1, %2 sc1\n\ts_mov_b64 exec, -1\n\ts_nop 0" ::"v"(lo), "v"(u32x4{r.x, r.y, z, (w & 0xffffu) | tag()}), "s"(ws + WS_KVF + (hp * 16 + rb) * KST) : "memory");
        STAMP(4);
        idle_wait<11 * 16>(fb_);
#pragma unroll
        for (int m = 0; m < 21; m++) ldwo(m);
      } else {
        __builtin_amdgcn_s_setprio(1);
        arrive_wait<15 * 16>(fb_, 0u);
        gptr wg = P_wo + (12 - wave) * 28672;
        u32 wl = sm0 + (12 - wave) * 7168;
        asm volatile("" : "+s"(wg), "+s"(wl));
        constexpr int NP = 28;
        static_for<0, NP>([&](auto I) {
          constexpr int i = I.v;
          if (i == NB1) { hints(); STAMP(4); if (D_W >= 0) until(th[0] + D_W); }
          if (i == NP - 7) idle_wait<11 * 16>(fb_);
          if (WOP < 60 && i >= WOP0) WAITV(WOP);
          dma16(wg - i / 7 * (3 * 28672), owo[i % 7], wl - i / 7 * (3 * 7168) + i % 7 * 1024);
          if (i == 13) dma_mark(pc, sm0 + L_FULL + 12 * 16 + wave * 4);
          if constexpr (i >= NP - 7) { ldwo(3 * (i - NP + 7)); ldwo(3 * (i - NP + 7) + 1); ldwo(3 * (i - NP + 7) + 2); }
        });
        dma_mark(pc, sm0 + L_FULL + 18 * 16 + wave * 4);
        STAMP(5);
      }
      __builtin_amdgcn_s_setprio(0);
    }
    const u32 tg = rfl(tag());
#pragma unroll
    for (int i = 0; i < 4; i++) { so_pq[i] = WS_PQ + ((rt * 4 + lw) * 16 + 12 - 4 * i + hf) * 1024; asm volatile("" : "+s"(so_pq[i])); }
    u32x4 pq[8], ka[4][4];
    auto ldpq = [&]() {
#pragma unroll
      for (int i = 0; i < 8; i++) { pq[i] = __builtin_amdgcn_raw_buffer_load_b128(wsr, lo + (i & 1) * 2048, so_pq[i >> 1], 16); SCHED(); }
    };
    auto dec_pq = [&]() {
      u32 bad = 0;
#pragma unroll
      for (int i = 0; i < 8; i++) { pq[i].w ^= tg; bad |= pq[i].w; }
      return __any(bad > 0xffffu);
    };
    auto ldka = [&](int j, int i) { if ((EXP & 32) && j >= 2) ka[j][i] = pq[i]; else ka[j][i] = *(LAS u32x4*)(sm + kr + (3 - j) * 8192 + (i >> 1) * 4096 + (i & 1) * 1024); SCHED(); };
    auto prewo = [&]() __attribute__((always_inline)) {
      idle_wait<18 * 16>(fa_);
#pragma unroll
      for (int m = 21; m < NPRE; m++) ldwo(m);
    };
    const u32 pb_ = L_O + 32768 + lw * 7168;
    u32 ps = pb_ + lo, pt = pb_ + hs * 4096 + l15 * 144 + g * 8, pr = pb_ + hs * 4096 + (lane >> 3) * 144 + (lane & 7) * 16;
    u32 fp = sm0 + L_FULL + (wave ^ 4) * 4, fw = L_FULL + 21 * 16 + wave * 4, one = 1u;
    f32x4 ce = {0.f, 0.f, 0.f, 1e-6f};
    gptr op1 = P_out + 8 * 1792;
    asm volatile("" : "+s"(op1));
    asm volatile("" : "+v"(ps), "+v"(pt), "+v"(pr), "+v"(fp), "+v"(fw), "+v"(one), "+v"(ce));
    const gptr kg = ws + WS_KVF + lw * (32 * KST);
    u32 ko = g * KST + l15 * 16;
    pin(ko);
    auto ldkv = [&]() __attribute__((always_inline)) {
#pragma unroll
      for (int i = 0; i < 8; i++) dma16c(kg + i * (4 * KST), ko, sm0 + L_O + lw * 8192 + i * 1024);
    };
    u32 kt[8], kv_ = kr + lw * 8192, fk;
    pin(kv_);
    auto rdkv = [&]() __attribute__((always_inline)) {
#pragma unroll
      for (int i = 0; i < 8; i++) kt[i] = *(volatile LAS u32*)(sm + kv_ + i * 1024 + 12);
    };
    auto badkv = [&]() __attribute__((always_inline)) {
      u32 bad = 0;
#pragma unroll
      for (int i = 0; i < 8; i++) bad |= kt[i] ^ tg;
      return __any(bad > 0xffffu);
    };
    auto ld1 = [&](int i) __attribute__((always_inline)) { pq[i] = __builtin_amdgcn_raw_buffer_load_b128(wsr, lo + (i & 1) * 2048, so_pq[i >> 1], 16); SCHED(); };
    STAMP(6);
    if (!hf) {
      idle_wait<13 * 16>(fa_);
      until(th[1]);
      ldkv();
    }
    const bool early = !DYN || __all(*(volatile LAS u32*)(uintptr_t)(fa_ + 18 * 16) != 0);
    if (early) {
      if (!DYN) idle_wait<18 * 16>(fa_);
      static_for<21, NPRE>([&](auto M) { ldwo(M.v); });
    }
    until(th[2]);
    __builtin_amdgcn_s_setprio(PRF);
#pragma unroll
    for (int i = 0; i < KV0; i++) {
      if (i == 7) { fk = *(volatile LAS u32*)(uintptr_t)(fb_ + 19 * 16); SCHED(); }
      ld1(i);
    }
    if (!hf) {
      WAITV(KV0);
      rdkv();
      SCHED();
#pragma unroll
      for (int i = KV0; i < KV1; i++) ld1(i);
      if (__builtin_expect(badkv(), 0)) {
#pragma unroll 1
        for (u32 spin = 0; spin < SPIN_CAP; spin++) {
          CNT();
          __builtin_amdgcn_s_sleep(RSL);
          ldkv();
          WAITV(0);
          rdkv();
          if (!badkv()) break;
        }
      }
      *(volatile LAS u32*)(sm + fw - 16) = one;
    } else {
#pragma unroll
      for (int i = KV0; i < KV1; i++) ld1(i);
    }
#pragma unroll
    for (int i = KV1; i < 8; i++) {
      if (i == 7) { fk = *(volatile LAS u32*)(uintptr_t)(fb_ + 19 * 16); SCHED(); }
      ld1(i);
    }
    STAMP(7);
    if (!early) prewo();
    arrive_wait<19 * 16>(fb_, fk);
    STAMP(8);
#pragma unroll
    for (int i = 0; i < 8; i++) ldka(i >> 2, (0x2031 >> (i & 3) * 4) & 3);
    if (__builtin_expect(dec_pq(), 0)) {
#pragma unroll 1
      for (u32 spin = 0; spin < SPIN_CAP; spin++) {
        CNT();
        __builtin_amdgcn_s_sleep(RSL);
        ldpq();
        if (!dec_pq()) break;
      }
    }
    SCHED();
    STAMP(10);
    __builtin_amdgcn_s_setprio(PRS & 3);
    f32x4 na[2][2], nb[2][2], oc[7];
    u32 of[7][4];
    float r0, r1, t0[4], t1[4];
    u32 fl;
    auto s1 = [&](int j, int i) {
      if (i < 2) nb[j & 1][i] = mfma16(ka[j][2 * i + 1], pq[2 * j + i], ce);
      else na[j & 1][i - 2] = mfma16(ka[j][2 * i - 4], pq[2 * j + i - 2], f32x4{0.f, 0.f, 0.f, 0.f});
      SCHED();
    };
    auto s2 = [&](int m) {
      const int j = mk(m);
      oc[mc(m)] = mfma16(wo[(EXP & 32) && m >= NPRE ? m - 16 : m], u32x4{of[j][0], of[j][1], of[j][2], of[j][3]}, m < 7 ? f32x4{0.f, 0.f, 0.f, 0.f} : oc[mc(m)]);
      SCHED();
      if (m + NPRE < 49 && !(EXP & 32)) ldwo(m + NPRE);
    };
    auto va = [&](int j_, int n) {
      const f32x4 *a = na[j_ & 1], *b = nb[j_ & 1];
      const int j = 3 - j_;
      auto mul = [&](float& d, float x, float r) { d = x * r; SCHED(); };
      auto cvt = [&](u32& d, float x, float y) { d = pk(x, y); SCHED(); };
      if (n == 0) { r0 = __builtin_amdgcn_rcpf(b[0][3]); r1 = __builtin_amdgcn_rcpf(b[1][3]); }
      if (n == 1) { mul(t0[0], b[0][0], r0); mul(t0[1], b[0][1], r0); }
      if (n == 2) { mul(t1[0], b[1][0], r1); cvt(of[4 + (j >> 1)][2 * (j & 1)], t0[0], t0[1]); }
      if (n == 3) { mul(t1[1], b[1][1], r1); mul(t0[2], b[0][2], r0); }
      if (n == 4) { cvt(of[4 + (j >> 1)][2 * (j & 1) + 1], t1[0], t1[1]); mul(t1[2], b[1][2], r1); }
      if (n == 5) { mul(t0[0], a[0][0], r0); cvt(of[6][j], t0[2], t1[2]); }
      if (n == 6) { mul(t0[1], a[0][1], r0); mul(t0[2], a[0][2], r0); }
      if (n == 7) { cvt(of[j][0], t0[0], t0[1]); mul(t0[3], a[0][3], r0); }
      if (n == 8) { mul(t1[0], a[1][0], r1); cvt(of[j][1], t0[2], t0[3]); }
      if (n == 9) { mul(t1[1], a[1][1], r1); mul(t1[2], a[1][2], r1); }
      if (n == 10) { cvt(of[j][2], t1[0], t1[1]); mul(t1[3], a[1][3], r1); }
      if (n == 11) cvt(of[j][3], t1[2], t1[3]);
      SCHED();
    };
#pragma unroll
    for (int i = 0; i < 4; i++) s1(0, i);
    static_for<0, 48>([&](auto N) {
      constexpr int j = N.v / 12, n = N.v % 12;
      if constexpr (n == 0 && j > 0 && (((PRS >> 4 * j) ^ (PRS >> (4 * j - 4))) & 3)) __builtin_amdgcn_s_setprio((PRS >> 4 * j) & 3);
      if constexpr (N.v == WBP) asm volatile("s_cmp_lg_u32 %0, %1\n\ts_cbranch_scc1 1f\n\tbuffer_wbl2 sc1\n1:" ::"s"(idx * 8 + wave), "n"(WBW) : "scc");
      va(j, n);
      if constexpr (j == 0) { if constexpr (n % 3 == 0) s1(1, n / 3); if constexpr (n >= 1 && n < 5) ldka(2, (0x2031 >> (n - 1) * 4) & 3); }
      if constexpr (j == 1) { if constexpr (n < 4) s1(2, n); else if constexpr (n < 11) s2(n - 4); if constexpr (n >= 4 && n < 8) ldka(3, (0x2031 >> (n - 4) * 4) & 3); }
      if constexpr (j == 2) { if constexpr (n < 4) s2(7 + n); else if constexpr (n < 8) s1(3, n - 4); else s2(3 + n); if constexpr (n == 11) s2(15); }
      if constexpr (j == 3) s2(16 + n);
    });
#pragma unroll
    for (int i = 0; i < 4; i++) asm volatile("" : "+v"(of[0][i]), "+v"(of[4][i]), "+v"(of[6][i]));
    STAMP(9);
    __builtin_amdgcn_s_setprio((PRS >> 16) & 3);
    f32x4 pp[4];
    u32 ob[2];
    auto put = [&](int c) { *(LAS f32x4*)(sm + ps + c * 1024) = oc[c]; SCHED(); };
    auto fo = [&](int c, int n) {
      if (n % 3 < 2) { const int i = n - n / 3; oc[c][i] += pp[c & 3][i]; }
      else {
        ob[n / 3] = pk(oc[c][n / 3 * 2], oc[c][n / 3 * 2 + 1]);
        if (n == 5) *(LAS u32x2*)(sm + pt + (c & 3) * 32) = u32x2{ob[0], ob[1]};
      }
      SCHED();
    };
    auto get = [&](int c0, int n) {
      arrive_wait<21 * 16>(fp, fl);
      RSTAMP(6);
#pragma unroll
      for (int c = 0; c < n; c++) { pp[c] = *(LAS f32x4*)(sm + ps + (c0 + c) * 1024); SCHED(); }
    };
    if (!hs) {
#pragma unroll
      for (int i = 0; i < 21; i++) {
        s2(28 + (i < 9 ? i / 3 * 7 + 4 + i % 3 : (i - 9) % 6 / 2 * 7 + (i - 9) / 6 * 2 + !(i & 1)));
        if (i >= 10 && i < 13) put(i - 6);
        if (i == 12) { *(volatile LAS u32*)(sm + fw) = one; SCHED(); }
        if (i == PG0 - 3) { fl = *(volatile LAS u32*)(uintptr_t)(fp + 21 * 16); SCHED(); }
        if (i == PG0) get(0, 4);
        if (i > 20 - FI) { const int j = (i - 21 + FI) * 3; fo(j / 6, j % 6); fo(j / 6, j % 6 + 1); fo(j / 6, j % 6 + 2); }
      }
      __builtin_amdgcn_s_setprio(3);
#pragma unroll
      for (int i = 3 * FI; i < 24; i++) fo(i / 6, i % 6);
    } else {
#pragma unroll
      for (int i = 0; i < 21; i++) {
        s2(28 + (i < 18 ? i % 6 / 2 * 7 + i / 6 * 2 + (i & 1) : (i - 18) * 7 + 6));
        if (i == 8 || i == 9) put(i - 8);
        if (i == 14 || i == 15) put(i - 12);
        if (i == 15) { *(volatile LAS u32*)(sm + fw) = one; SCHED(); }
        if (i == PG1 - 3) { fl = *(volatile LAS u32*)(uintptr_t)(fp + 21 * 16); SCHED(); }
        if (i == PG1) get(4, 3);
      }
      __builtin_amdgcn_s_setprio(3);
#pragma unroll
      for (int i = 0; i < 18; i++) fo(4 + i / 6, i % 6);
    }
    const u32x4 o0 = *(LAS u32x4*)(sm + pr), o1 = *(LAS u32x4*)(sm + pr + 8 * 144);
    RSTAMP(7);
    if ((lane & 7) < 8 - 2 * hs) {
      asm volatile("global_store_dwordx4 %0, %1, %2" OUTM ::"v"(oo), "v"(o0), "s"(P_out) : "memory");
      asm volatile("global_store_dwordx4 %0, %1, %2" OUTM ::"v"(oo), "v"(o1), "s"(op1) : "memory");
    }
  } else if (__builtin_expect(xc != XS, 1)) {
    const int par = wave >> 2;
    u32 o = (u32)(mu * 128 + wave * 16 + (lane >> 3)) * 896u + (u32)gam * 128u + (u32)(lane & 7) * 16u;
    auto ldu = [&]() {
#pragma unroll
      for (int i = 0; i < 2; i++) dma16n(P_u + i * (8 * 896), o, sm0 + A_K + lw * K_PITCH + 256 + par * 2048 + i * 1024);
      dma4n(P_dskip, pre[2], sm0 + pl + 7 * 256);
    };
    if (!par && UH < 0) ldu();
    {
      const u32 za = lane < 29 ? A_K + lw * K_PITCH + lane * 8 : lane < 33 ? A_U + lw * U_PITCH + (lane - 29) * 8 : lane < 41 ? A_CZ + (lane - 33) * 8 : MISC;
      asm volatile("ds_write_b64 %0, %1" ::"v"(sm0 + za), "v"(u32x2{0u, 0u}) : "memory");
    }
    BAR();
    gptr tp = P_wo + min((u32)(tid * 16 + jj), 3135u) * 128u;
    if (UH >= 0) { if (!((M0 >> wave) & 1)) touch(ba, sm0 + JUNK); pin(o); } else
    if (par) {
      const u32 b2 = (TORD >> (4 * lw)) & 15u;
      const gptr ba2 = (lane < 32 && b2 < 7 ? P_x : P_wqkv) + bline(b2, lane, jj, xc);
      const bool first = (M0 >> wave) & 1;
      if (UPOS == 0) ldu();
      touch(first ? ba2 : ba, sm0 + JUNK);
      if (UPOS == 1) ldu();
      if (QLATE && !first) tp = ba2;
      if (!QLATE && !first) touch(ba2, sm0 + JUNK);
    }
    pin(tp);
    epoch_ld();
    PSTAMP(7);
    const u32 sgo = par * KG_GRP + lw * 256;
#define PUT(row, v) asm volatile("ds_write_addtid_b32 %0 offset:%1" ::"v"(v), "n"((row) * KG_PITCH) : "memory")
#define SETM0() set_m0(sm0 + sgo)
    SETM0();
    PUT(0, 0x3c00u);
    PSTAMP(8);
    asm volatile("s_setreg_imm32_b32 hwreg(HW_REG_MODE, 4, 2), 0\n\ts_nop 1\n\ts_setreg_imm32_b32 hwreg(HW_REG_MODE, 9, 1), 0");
    u32 fr_ = sgo + (lane & 31) * KG_PITCH + (lane >> 5) * 16, tr_ = A_K + lw * K_PITCH + 256 + par * 2048 + (8 * (p & 1) + q) * 128 + (2 * g + (p >> 1)) * 8;
    u32 uo = (u32)((2 * g + (q >> 1)) * 16384 + (lane & 7) * 16), lo = lane * 16;
    gptr fp = ws + WS_FLG + (gam * 16 + wave * 2 + (lane & 1)) * FS, utp = ws + WS_UT + gam * 16 * 16384 + (8 * mu + wave) * 128;
    asm volatile("" : "+v"(uo), "+v"(fr_), "+v"(tr_), "+v"(fp), "+v"(lo));
    asm volatile("" : "+s"(utp));
    u32 xr = (KG_GRP - par * KG_GRP) + lw * 256 + lane * 4, uw = A_U + 32 + p * U_PITCH + (wave * 32 + (lane >> 2)) * 16;
    u32 e8 = A_K + lw * K_PITCH + 232 + 16 * (lane & 31) + (lane >> 5) * 8, e8m = e8 - (lane ? 2 : 0), e512 = e8 + (lane >> 5) * 504, e1k = e512 + (lane >> 5) * 512;
    u32 e1kf = e1k + ((lane & 1) ? 0 : 8);
    asm volatile("" : "+v"(xr), "+v"(uw), "+v"(e8), "+v"(e8m), "+v"(e512), "+v"(e1k), "+v"(e1kf));
    u32 ub = A_U + lw * U_PITCH + 8 * (1 + q + p + 8 * g), kb = A_K + lw * K_PITCH + 224 + 32 * p - 64 * g - 8 * q + 128 * par;
    u32 cw = sm0 + A_CZ - 64 * (31 + par) + (63 - lane + lw) * 4;
    u32 prd = A_PL + tid * 4, crd = sm0 + (lane < 3 ? A_CZ - 64 * wave + lane * 16 : A_CZ), cst = lane < 3 ? 512 : 0;
    u32 yo = (u32)tid * 1792u + (u32)an * 8u;
    u64 l63 = 0x8880000000000000ull;
    gptr outp[4];
#pragma unroll
    for (int i = 0; i < 4; i++) { outp[i] = P_out + i * (512 * 1792); asm volatile("" : "+s"(outp[i])); }
    asm volatile("" : "+v"(ub), "+v"(kb), "+v"(cw), "+v"(prd), "+v"(crd), "+v"(cst), "+v"(yo));
    asm volatile("" : "+s"(l63));
    LAS char* fr = sm + fr_;
    __builtin_amdgcn_s_setprio(3);
    SCHED();
    PSTAMP(10);
    epoch_get();
    if (UH >= 0) { if ((M0 >> wave) & 1) WAITV(0); else WAITV(1); } else
    if (!par) WAITV(3); else if (QLATE || ((M0 >> wave) & 1)) WAITV(4); else WAITV(5);
    u32 pr[6];
    const u32 ldt_ = *(LAS u32*)(sm + pl + 6 * 256 + lane * 4);
#pragma unroll
    for (int i = 0; i < 6; i++) pr[i] = *(LAS u32*)(sm + pl + i * 256 + lane * 4);
    float dt;
    {
      const float x = bf2f(ldt_ >> (lw & 1) * 16), hi = x * 1.44269504f, lo = __builtin_fmaf(x, 1.92596299e-8f, __builtin_fmaf(x, 1.44269504f, -hi));
      const float e = __builtin_amdgcn_exp2f(hi);
      dt = bf2f(pk(__builtin_fmaf(e, lo * 0.693147181f, e), 0.f) & 0xffffu);
    }
    asm volatile("" : "+v"(pr[0]), "+v"(pr[1]), "+v"(pr[2]), "+v"(pr[3]), "+v"(pr[4]), "+v"(pr[5]), "+v"(dt));
    STAMP(1);
    float ar[2], ai[2], cr[2], ci[2];
#pragma unroll
    for (int r = 0; r < 2; r++) {
      float f[6];
#pragma unroll
      for (int i = 0; i < 6; i++) f[i] = r ? bfhi(pr[i]) : bf2f(pr[i]);
      ar[r] = (f[0] * dt) * 1.44269504088896f; ai[r] = (f[1] * dt) * 0.159154943091895f;
      cr[r] = f[4] * f[2] - f[5] * f[3]; ci[r] = f[4] * f[3] + f[5] * f[2];
    }
    u32 mx = max(__builtin_bit_cast(u32, fmaxf(fmaxf(fabsf(cr[0]), fabsf(ci[0])), fmaxf(fabsf(cr[1]), fabsf(ci[1])))), 0x01000000u);
    const u32 ex = min(wave_max(mx) & 0x7f800000u, 0x7e000000u);
    const float csc = __uint_as_float(0x7f000000u - ex), ks = dt * __uint_as_float(ex);
    const u32 chf[2] = {pkh(cr[0] * csc, -ci[0] * csc), pkh(cr[1] * csc, -ci[1] * csc)};
    f32x16 kacc;
#pragma unroll
    for (int i = 0; i < 16; i++) kacc[i] = 0.f;
    u32x4 fa[8], fb[8], uv[2];
    u64 fv;
    u32 dsk_;
    auto hook = [&](auto N) {
      constexpr int n = N.v;
      if constexpr (n == H1POS) {
        if (UH >= 0) WAITV(TP3 > UH && TP3 < H1POS); else
        if (!par) WAITV(0); else if (QLATE || ((M0 >> wave) & 1)) WAITV(1 - UPOS); else WAITV(2 - UPOS);
#pragma unroll
        for (int k = 0; k < 2; k++) uv[k] = trd2(sm + tr_ + 64 * k, sm + tr_ + 64 * k + 512);
        dsk_ = *(LAS u32*)(sm + pl + 7 * 256);
        SCHED();
      }
      if constexpr (n == H1POS + 6) {
#pragma unroll
        for (int k = 0; k < 2; k++) st16(utp + k * (8 * 16384), uo, uv[k]);
        pin(dsk_);
        STAMP(5);
        SCHED();
      }
      if constexpr (n == UH) { ldu(); SETM0(); SCHED(); }
      if constexpr (n == TP3) { touch(tp, sm0 + JUNK); SETM0(); SCHED(); }
      if constexpr (n == H2POS) {
        WAITV(TP3 > H1POS + 6 && TP3 < H2POS);
        if (last() && lane == 0) st8(ws + WS_FLG + an * FS, T);
        STAMP(6);
        SCHED();
      }
      if constexpr (n == H3POS) { fv = ld8(fp); SCHED(); }
      if constexpr (n == H4POS) {
        if (!__all(fv == T)) { CNT(); poll(fp, T); }
        STAMP(7);
#pragma unroll
        for (int k = 0; k < 2; k++) uv[k] = __builtin_amdgcn_raw_buffer_load_b128(wsr, lo + k * 1024, WS_UT + an * 16384 + wave * 2048, 16);
        SCHED();
      }
    };
    static_for<0, 2>([&](auto R) {
      constexpr int r = R.v;
      float px[11], py[11];
      u32 h[72], t[NOPS];
      auto zpow = [&](int k, float m) {
        const float e = __builtin_amdgcn_exp2f(ar[r] * m), a = __builtin_amdgcn_fractf(ai[r] * m);
        px[k] = __builtin_amdgcn_cosf(a) * e; py[k] = __builtin_amdgcn_sinf(a) * e;
      };
      h[32] = chf[r];
      zpow(8, 256.f); zpow(0, 1.f);
      PUT(32, h[32]);
      SCHED();
      static_for<0, NOPS + 2>([&](auto I) {
        constexpr int i = I.v;
        if constexpr (i < NOPS) {
          constexpr Op o = OPS[i];
          if constexpr (o.op == 0) t[i] = hm1(h[o.a], h[o.b]);
          if constexpr (o.op == 1) asm("v_mul_f32 %0, %1, %2 mul:2" : "=v"(py[o.a + 1]) : "v"(px[o.a]), "v"(py[o.a]));
          if constexpr (o.op == 2) { h[P0 + o.a] = pkh(px[o.a], py[o.a]); if constexpr (o.d >= 0) h[o.d] = h[P0 + o.a]; }
          SCHED();
        }
        if constexpr (i >= 2) {
          constexpr Op o = OPS[i - 2];
          if constexpr (o.op != 1 && o.d >= 0) { PUT(o.d, h[o.d]); SCHED(); }
        }
        if constexpr (i >= 1 && i <= NOPS) {
          constexpr Op o = OPS[i - 1];
          if constexpr (o.op == 0) h[o.d] = hm2<(o.d >= 32)>(h[o.a], h[o.b], t[i - 1]);
          if constexpr (o.op == 1) px[o.a + 1] = __builtin_fmaf(px[o.a], px[o.a], -(py[o.a] * py[o.a]));
          SCHED();
        }
        if constexpr (r == 1 && i == 3) { asm volatile("" ::"v"(fb[7])); SCHED(); }
        if constexpr (r == 1 && i >= 4 && i < 36 && (i & 3) == 0) { kacc = mfma32h(fa[i / 4 - 1], fb[i / 4 - 1], kacc); SCHED(); }
        if constexpr (i >= 44 && i < 68 && (i - 44) % 3 == 0) { fa[(i - 44) / 3] = *(LAS u32x4*)(fr + 32 * KG_PITCH + (i - 44) / 3 * 32); SCHED(); }
        if constexpr (100 * r + i == KW1) __builtin_amdgcn_s_setprio(2);
        if constexpr (100 * r + i == KW2) __builtin_amdgcn_s_setprio(1);
        if constexpr (100 * r + i == KW3) __builtin_amdgcn_s_setprio(0);
        hook(IC<100 * r + i>{});
      });
#pragma unroll
      for (int k = 0; k < 8; k++) fb[k] = *(LAS u32x4*)(fr + k * 32);
      SCHED();
    });
#pragma unroll
    for (int k = 0; k < 8; k++) kacc = mfma32h(fa[k], fb[k], kacc);
    __builtin_amdgcn_s_setprio(0);
    if (par) {
#pragma unroll
      for (int i = 0; i < 6; i++) PUT(i, kacc[i] * ks);
    } else {
#pragma unroll
      for (int i = 0; i < 5; i++) PUT(i, kacc[7 + i] * ks);
    }
    STAMP(2);
    hook(IC<200>{});
    BAR();
    STAMP(3);
    float kx[6];
#pragma unroll
    for (int i = 0; i < 6; i++) kx[i] = *(LAS float*)(sm + xr + i * KG_PITCH);
    SCHED();
    auto run8 = [&](float v, int base) {
      const u32 vv = pk(v, v);
      *(LAS u32x4*)(sm + e1k + 2 * base - 8) = u32x4{vv, vv, vv, vv};
      asm volatile("" ::: "memory");
      *(LAS u16*)(sm + e1kf + 2 * base) = (u16)vv;
      asm volatile("" ::: "memory");
    };
    if (par) {
#pragma unroll
      for (int i = 0; i < 5; i++) kx[i] = __builtin_fmaf(kacc[7 + i], ks, kx[i]);
      run8(kx[0], 1024);
      const u32 aa = pk(kx[3], kx[3]);
      *(LAS u32*)(sm + e512 + 1020) = aa;
      *(LAS u32x2*)(sm + e512 + 1024) = u32x2{aa, pk(kx[3], kx[4])};
      *(LAS u32*)(sm + e512 + 1032) = pk(kx[4], kx[4]);
      asm volatile("" ::: "memory");
      const u32 bb = pk(kx[1], kx[1]);
      *(LAS u32*)(sm + e8 + 512) = bb;
      *(LAS u16*)(sm + e8 + 516) = (u16)pk(kx[2], kx[2]);
      *(LAS u16*)(sm + e8m + 512) = (u16)bb;
    } else {
#pragma unroll
      for (int i = 0; i < 6; i++) kx[i] = __builtin_fmaf(kacc[i], ks, kx[i]);
      if (lane == 0) kx[0] += bf2f(dsk_ >> (lw & 1) * 16);
      *(LAS u32x2*)(sm + e8) = u32x2{pk(kx[0], kx[1]), pk(kx[2], kx[3])};
      run8(kx[4], 1280);
      if (lane == 0) { const u32 vv = pk(kx[5], kx[5]); *(LAS u32x2*)(sm + e8 + 2 * 2044) = u32x2{vv, vv}; }
    }
    SCHED();
    hook(IC<201>{});
#pragma unroll
    for (int k = 0; k < 2; k++) *(LAS u32x4*)(sm + uw + k * 256) = uv[k];
    BAR();
    STAMP(4);
    {
      const u64 m1 = rfl64(0ull - (u64)par), m0 = ~m1;
#define CONV_IN [ub] "v"(sm0 + ub), [kb] "v"(sm0 + kb), [pw] "s"(sm0 + A_PL + lw * 8192 + par * 256), [cw] "v"(cw), [prd] "v"(sm0 + prd), [cst] "v"(cst), [yo] "v"(yo), \
                [o0] "s"(outp[0]), [o1] "s"(outp[1]), [o2] "s"(outp[2]), [o3] "s"(outp[3]), [l63] "s"(l63), [m0] "s"(m0), [m1] "s"(m1)
      asm volatile(CONV_ASM : [crd] "+v"(crd) : CONV_IN : "memory", CONV_CLOB);
    }
    FINISH();
  } else {
    if (!((MS0 >> wave) & 1)) { __builtin_amdgcn_s_sleep(SSL); touch(ba, sm0 + JUNK); }
    WAITV(0);
    STAMP(1);
    if (wave < 4) touch(P_wo + min((u32)(tid * 16 + jj), 3135u) * 128u, sm0 + JUNK);
    if (idx == 31 && wave == 7) {
      epoch_ld();
      epoch_get();
      __builtin_amdgcn_s_sleep(EBS);
      const u32x4 z = {(u32)T + 1u, 0u, 0u, 0u};
      poll2(rep + lane * 16, z, (u32)T);
#pragma unroll 1
      for (int i = 0; i < 2; i++) poll(ws + WS_FLG + (i * 48 + lane) * FS, T);
      if (lane == 0) st8(ws + WS_EPOCH, T);
    }
    __builtin_amdgcn_s_waitcnt(0x0f70);
    FINISH();
  }
  if (ENDW) WAITV(0);
  STAMP(11);
  STAMP_OUT();
}

#if __HIP_DEVICE_COMPILE__
#pragma clang attribute pop
#endif
#undef __all
#undef __any
#undef __uint_as_float
#define WSOFF 0
extern "C" void kernel_launch(void* const* d_in, const int* in_sizes, int n_in, void* d_out, int out_size, void* d_ws,
                              size_t ws_size, hipStream_t stream) {
  typedef const char* cp;
  fused_kernel<<<256, 512, 0, stream>>>((cp)d_in[0], (cp)d_in[1], (cp)d_in[2], (cp)d_in[3], (cp)d_in[4], (cp)d_in[5], (cp)d_in[6],
                                        (cp)d_in[7], (cp)d_in[8], (cp)d_in[9], (cp)d_in[10], (cp)d_in[11], (char*)d_out, (char*)d_ws + WSOFF);
}
```
